```python
import torch

def _softmax(x, axis=-1):
    return torch.softmax(x, dim=axis)
def _gelu(x):
    return torch.nn.functional.gelu(x, approximate="tanh")
def _silu(x):
    return torch.nn.functional.silu(x)
def _sigmoid(x):
    return torch.sigmoid(x)
def _layernorm(x, axis=-1, eps=1e-5):
    m = x.mean(dim=axis, keepdim=True)
    v = x.var(dim=axis, keepdim=True, unbiased=False)
    return (x - m) / torch.sqrt(v + eps)
def _scatter_add(tgt, idx, src, axis=0):
    return tgt.clone().index_add_(axis, idx, src)
def _segment_sum(src, seg, n):
    out = torch.zeros((n,) + src.shape[1:], dtype=src.dtype)
    return out.index_add_(0, seg, src)
def _pad(x, pw):
    flat = tuple(v for pair in reversed(pw) for v in pair)
    return torch.nn.functional.pad(x, flat)

kernel_name = 'gated_mix__gqa_block_cached__gqa_block_cached__decode'

def setup_inputs():

    _w = (lambda a: (lambda t: t.to(torch.bfloat16) if t.is_floating_point() else t)(torch.as_tensor(a)))
    def _A_setup():
        B, Sq, D = 4, 1, 1024
        Hq, Hkv, Sk, Dh = 32, 4, 8192, 128
        assert Hq % Hkv == 0, (Hq, Hkv)
        return {
            "x": _w((lambda *s: torch.randn(*s, dtype=torch.bfloat16) * 0.05)(B, Sq, D)),
            "k_cache": _w((lambda *s: torch.randn(*s, dtype=torch.bfloat16) * 6.4)(B, Hkv, Sk, Dh)),
            "v_cache": _w((lambda *s: torch.randn(*s, dtype=torch.bfloat16) * 0.05)(B, Hkv, Sk, Dh)),
            "wq": _w((lambda *s: torch.randn(*s, dtype=torch.bfloat16) * 6.4)(D, Hq * Dh) * (D ** -0.5)),
            "wo": _w((lambda *s: torch.randn(*s, dtype=torch.bfloat16) * 0.05)(Hq * Dh, D) * ((Hq * Dh) ** -0.5)),
        }
    _A_in = _A_setup()

    def _B_setup():
        B, Sq, D = 4, 1, 1024
        Hq, Hkv, Sk, Dh = 32, 4, 8192, 128
        assert Hq % Hkv == 0, (Hq, Hkv)
        return {
            "x": _w((lambda *s: torch.randn(*s, dtype=torch.bfloat16) * 0.05)(B, Sq, D)),
            "k_cache": _w((lambda *s: torch.randn(*s, dtype=torch.bfloat16) * 6.4)(B, Hkv, Sk, Dh)),
            "v_cache": _w((lambda *s: torch.randn(*s, dtype=torch.bfloat16) * 0.05)(B, Hkv, Sk, Dh)),
            "wq": _w((lambda *s: torch.randn(*s, dtype=torch.bfloat16) * 6.4)(D, Hq * Dh) * (D ** -0.5)),
            "wo": _w((lambda *s: torch.randn(*s, dtype=torch.bfloat16) * 0.05)(Hq * Dh, D) * ((Hq * Dh) ** -0.5)),
        }
    _B_in = _B_setup()

    return {
        "A_x": _A_in["x"],
        "A_k_cache": _A_in["k_cache"],
        "A_v_cache": _A_in["v_cache"],
        "A_wq": _A_in["wq"],
        "A_wo": _A_in["wo"],
        "B_x": _B_in["x"],
        "B_k_cache": _B_in["k_cache"],
        "B_v_cache": _B_in["v_cache"],
        "B_wq": _B_in["wq"],
        "B_wo": _B_in["wo"],
        "gate_w": _w((lambda *s: torch.randn(*s, dtype=torch.bfloat16) * 0.05)(1024) * 16.0),
    }

def reference(A_x, A_k_cache, A_v_cache, A_wq, A_wo, B_x, B_k_cache, B_v_cache, B_wq, B_wo, gate_w):
    def _A_ref(x, k_cache, v_cache, wq, wo):
        B, Sq, D = x.shape
        Hq, Dh = 32, 128
        Hkv = k_cache.shape[1]
        rep = Hq // Hkv
        q = (lambda x, *d: x.permute(*d))((x @ wq).reshape(B, Sq, Hq, Dh), 0, 2, 1, 3)
        k = (lambda x, n, axis: torch.repeat_interleave(x, n, dim=axis))(k_cache, rep, axis=1)
        v = (lambda x, n, axis: torch.repeat_interleave(x, n, dim=axis))(v_cache, rep, axis=1)
        scores = torch.einsum("bhqd,bhkd->bhqk", q, k) * (Dh ** -0.5)
        p = _softmax(scores, axis=-1)
        o = torch.einsum("bhqk,bhkd->bhqd", p, v)
        return (lambda x, *d: x.permute(*d))(o, 0, 2, 1, 3).reshape(B, Sq, Hq * Dh) @ wo

    def _B_ref(x, k_cache, v_cache, wq, wo):
        B, Sq, D = x.shape
        Hq, Dh = 32, 128
        Hkv = k_cache.shape[1]
        rep = Hq // Hkv
        q = (lambda x, *d: x.permute(*d))((x @ wq).reshape(B, Sq, Hq, Dh), 0, 2, 1, 3)
        k = (lambda x, n, axis: torch.repeat_interleave(x, n, dim=axis))(k_cache, rep, axis=1)
        v = (lambda x, n, axis: torch.repeat_interleave(x, n, dim=axis))(v_cache, rep, axis=1)
        scores = torch.einsum("bhqd,bhkd->bhqk", q, k) * (Dh ** -0.5)
        p = _softmax(scores, axis=-1)
        o = torch.einsum("bhqk,bhkd->bhqd", p, v)
        return (lambda x, *d: x.permute(*d))(o, 0, 2, 1, 3).reshape(B, Sq, Hq * Dh) @ wo

    x = A_x
    g = _sigmoid((x * gate_w).sum(-1, keepdims=True))
    a = _A_ref(x, A_k_cache, A_v_cache, A_wq, A_wo)
    b = _B_ref(x, B_k_cache, B_v_cache, B_wq, B_wo)
    return g * a + (1 - g) * b
```

```cpp
#include <hip/hip_runtime.h>
#include <hip/hip_bf16.h>
#include <stdint.h>

#define GAS __attribute__((address_space(1)))
#define LAS __attribute__((address_space(3)))
typedef unsigned v4u __attribute__((ext_vector_type(4)));
typedef unsigned v2u __attribute__((ext_vector_type(2)));
typedef float f32x4 __attribute__((ext_vector_type(4)));
typedef float f32x2 __attribute__((ext_vector_type(2)));
typedef __bf16 bf16x8 __attribute__((ext_vector_type(8)));
typedef GAS unsigned gu32;

#define KWIN 8
#define STAMPS 0

constexpr int DM = 1024, NQ = 4096, SK = 8192, DH = 128;
constexpr int NWG = 256, NTHR = 512;
constexpr int AUX_SC1 = 16;
constexpr unsigned SPIN_CAP = 1u << 20;

constexpr int WS_EPOCH = 0;
constexpr int WS_QX = 4096;
constexpr int WS_LX = WS_QX + 32 * 4096;
constexpr int WS_PX = WS_LX + 2 * 32 * 4 * 8 * 8;
constexpr int WS_OX = WS_PX + 256 * 8 * 1024;
constexpr int WS_END = WS_OX + 4 * 64 * 8192;
constexpr int WS_STAMP = WS_END;

constexpr int L_QPART = 0;
constexpr int L_OPART = 0;
constexpr int OP_STR = 132;
constexpr int L_WOP = 0;
constexpr int L_ML = 8 * 8 * OP_STR * 4;
constexpr int L_QS = L_ML + 8 * 8 * 2 * 4;
constexpr int L_ZP = L_QS + 2048;
constexpr int L_OB = L_ZP + 128;
constexpr int L_END = L_OB + 1024;

struct Params {
  const void* x;
  const void* kc0; const void* vc0; const void* wq0; const void* wo0;
  const void* kc1; const void* vc1; const void* wq1; const void* wo1;
  const void* gw;
  void* out;
  void* ws;
};

__device__ __forceinline__ v4u ldnt(const GAS unsigned char* p) { return __builtin_nontemporal_load((const GAS v4u*)p); }
__device__ __forceinline__ v4u ldpl(const GAS unsigned char* p) { return *(const GAS v4u*)p; }
__device__ __forceinline__ f32x4 mfma(v4u a, v4u b, f32x4 c) {
  return __builtin_amdgcn_mfma_f32_16x16x32_bf16(__builtin_bit_cast(bf16x8, a), __builtin_bit_cast(bf16x8, b), c, 0, 0, 0);
}
__device__ __forceinline__ unsigned perm_lo(unsigned a, unsigned b) { return __builtin_amdgcn_perm(b, a, 0x05040100u); }
__device__ __forceinline__ unsigned perm_hi(unsigned a, unsigned b) { return __builtin_amdgcn_perm(b, a, 0x07060302u); }
__device__ __forceinline__ float asf(unsigned u) { return __builtin_bit_cast(float, u); }
__device__ __forceinline__ unsigned asu(float f) { return __builtin_bit_cast(unsigned, f); }
__device__ __forceinline__ unsigned cvtpk(float lo, float hi) {
  typedef __bf16 bf16x2 __attribute__((ext_vector_type(2)));
  bf16x2 r; r[0] = (__bf16)lo; r[1] = (__bf16)hi;
  return __builtin_bit_cast(unsigned, r);
}
template <int CTRL> __device__ __forceinline__ float dpp(float x) {
  return __builtin_bit_cast(float, __builtin_amdgcn_mov_dpp(__builtin_bit_cast(int, x), CTRL, 0xf, 0xf, true));
}
constexpr int DPP_XOR1 = 0xB1, DPP_XOR2 = 0x4E, DPP_ROR8 = 0x128;
__device__ __forceinline__ float x16_max(float x) {
  auto s = __builtin_amdgcn_permlane16_swap(asu(x), asu(x), false, false);
  return fmaxf(asf(s[0]), asf(s[1]));
}
__device__ __forceinline__ float x32_max(float x) {
  auto s = __builtin_amdgcn_permlane32_swap(asu(x), asu(x), false, false);
  return fmaxf(asf(s[0]), asf(s[1]));
}
__device__ __forceinline__ float x16_sum(float x) {
  auto s = __builtin_amdgcn_permlane16_swap(asu(x), asu(x), false, false);
  return asf(s[0]) + asf(s[1]);
}
__device__ __forceinline__ float x32_sum(float x) {
  auto s = __builtin_amdgcn_permlane32_swap(asu(x), asu(x), false, false);
  return asf(s[0]) + asf(s[1]);
}
__device__ __forceinline__ void wg_barrier() {
  asm volatile("s_waitcnt lgkmcnt(0)" ::: "memory");
  __builtin_amdgcn_s_barrier();
  asm volatile("" ::: "memory");
}
#define SCHED_FENCE() __builtin_amdgcn_sched_barrier(0)

__device__ __forceinline__ void tr_mfma8(const v4u (&in)[8], v4u bfrag, f32x4 (&acc)[8]) {
#pragma unroll
  for (int t = 0; t < 4; ++t) {
    v4u lo, hi;
#pragma unroll
    for (int u = 0; u < 4; ++u) { lo[u] = perm_lo(in[2 * u][t], in[2 * u + 1][t]); hi[u] = perm_hi(in[2 * u][t], in[2 * u + 1][t]); }
    acc[2 * t] = mfma(lo, bfrag, acc[2 * t]);
    acc[2 * t + 1] = mfma(hi, bfrag, acc[2 * t + 1]);
  }
}

#define STAMP(i) do { } while (0)
#define STAMP_FLUSH() do { } while (0)

__global__ void __launch_bounds__(NTHR, 2) fused_kernel(Params p) {
  __shared__ __attribute__((aligned(16))) unsigned char smem[L_END];
  LAS unsigned char* L = (LAS unsigned char*)smem;
  const int tid = threadIdx.x, lane = tid & 63;
  const int w = __builtin_amdgcn_readfirstlane(tid >> 6);
  const int wg = blockIdx.x;
  const int br = wg >> 7, b = (wg >> 5) & 3, kvh = (wg >> 3) & 3, sp = wg & 7;
  const int g = lane >> 4, c = lane & 15;
  const GAS unsigned char* wsb = (const GAS unsigned char*)p.ws;
  const __amdgpu_buffer_rsrc_t rs = __builtin_amdgcn_make_buffer_rsrc(p.ws, 0, WS_END, 0x00020000);
  STAMP(0);

  const unsigned epoch = __hip_atomic_load((gu32*)p.ws, __ATOMIC_RELAXED, __HIP_MEMORY_SCOPE_AGENT);

  const GAS unsigned char* xq = (const GAS unsigned char*)p.x;
  const GAS unsigned char* wq = (const GAS unsigned char*)(br ? p.wq1 : p.wq0);
  const GAS unsigned char* wo = (const GAS unsigned char*)(br ? p.wo1 : p.wo0);
  const GAS unsigned char* kc = (const GAS unsigned char*)(br ? p.kc1 : p.kc0);
  const GAS unsigned char* vc = (const GAS unsigned char*)(br ? p.vc1 : p.vc0);
  const int xk = 128 * w + 32 * g + 8 * (c & 3);
  const v4u xf = ldpl(xq + ((c >> 2) * DM + xk) * 2);
  const v4u gf = ldpl((const GAS unsigned char*)p.gw + xk * 2);
  const int qj = sp * 4 + b;
  v4u wreg[8];
  {
    const GAS unsigned char* wp = wq + ((size_t)(128 * w + 8 * (lane >> 2)) * NQ + kvh * 1024 + qj * 32 + 8 * (lane & 3)) * 2;
#pragma unroll
    for (int i = 0; i < 8; ++i) wreg[i] = ldnt(wp + (size_t)i * NQ * 2);
  }
  const size_t kvbase = ((size_t)(b * 4 + kvh) * SK + sp * 1024 + w * 128) * (DH * 2);
  const GAS unsigned char* kp = kc + kvbase + (c >> 2) * 256 + g * 64 + (c & 3) * 16;
  const GAS unsigned char* vp = vc + kvbase + lane * 16;
  v4u kv[32];
#pragma unroll
  for (int i = 0; i < KWIN; ++i) kv[i] = ldnt(kp + i * 1024);
  SCHED_FENCE();

  {
    f32x4 acc[8];
#pragma unroll
    for (int j = 0; j < 8; ++j) acc[j] = f32x4{0.f, 0.f, 0.f, 0.f};
    tr_mfma8(wreg, xf, acc);
    if ((c & 3) == g) {
      LAS f32x4* qp = (LAS f32x4*)(L + L_QPART) + ((w * 4 + g) * 4 + (c >> 2)) * 8;
#pragma unroll
      for (int r = 0; r < 4; ++r) {
        qp[2 * r] = f32x4{acc[0][r], acc[1][r], acc[2][r], acc[3][r]};
        qp[2 * r + 1] = f32x4{acc[4][r], acc[5][r], acc[6][r], acc[7][r]};
      }
    }
    float z = 0.f;
#pragma unroll
    for (int t = 0; t < 4; ++t) {
      z = fmaf(asf(xf[t] << 16), asf(gf[t] << 16), z);
      z = fmaf(asf(xf[t] & 0xffff0000u), asf(gf[t] & 0xffff0000u), z);
    }
    z += dpp<DPP_XOR1>(z); z += dpp<DPP_XOR2>(z); z = x16_sum(z); z = x32_sum(z);
    if (g == 0 && (c & 3) == 0) ((LAS float*)(L + L_ZP))[w * 4 + (c >> 2)] = z;
  }
  STAMP(1);
  wg_barrier();
  const unsigned tag = __builtin_amdgcn_readfirstlane(epoch) + 1u;

  if (w == 0) {
    const LAS f32x2* qp = (const LAS f32x2*)(L + L_QPART) + g * 16 + c;
    float s0 = 0.f, s1 = 0.f;
#pragma unroll
    for (int u = 0; u < 32; ++u) { const f32x2 v = qp[u * 64]; s0 += v[0]; s1 += v[1]; }
    v2u gr; gr[0] = cvtpk(s0, s1); gr[1] = tag;
    const int off = WS_QX + (((br * 4 + kvh) * 4 + g) * 512 + qj * 16 + c) * 8;
    __builtin_amdgcn_raw_buffer_store_b64(gr, rs, off, 0, AUX_SC1);
  }
  STAMP(2);
  v4u qg[4];
  const int qoff = WS_QX + ((br * 4 + kvh) * 4 + b) * 4096 + lane * 16;
  constexpr int SWEEP_AT = 20;
#pragma unroll
  for (int i = KWIN; i < 32; ++i) {
    if (KWIN < 32) asm volatile("s_waitcnt vmcnt(%0)" ::"n"(KWIN - 1) : "memory");
    if (i == SWEEP_AT && w == 0) {
#pragma unroll
      for (int u = 0; u < 4; ++u) qg[u] = __builtin_amdgcn_raw_buffer_load_b128(rs, qoff + u * 1024, 0, AUX_SC1);
    }
    kv[i] = ldnt(kp + i * 1024);
    SCHED_FENCE();
  }
  STAMP(3);
  if (w == 0) {
    if (KWIN > SWEEP_AT) {
#pragma unroll
      for (int u = 0; u < 4; ++u) qg[u] = __builtin_amdgcn_raw_buffer_load_b128(rs, qoff + u * 1024, 0, AUX_SC1);
    }
    for (unsigned spins = 0; spins < SPIN_CAP; ++spins) {
      bool ok = true;
#pragma unroll
      for (int u = 0; u < 4; ++u) ok = ok && qg[u][1] == tag && qg[u][3] == tag;
      if (__all(ok)) break;
      __builtin_amdgcn_s_sleep(2);
#pragma unroll
      for (int u = 0; u < 4; ++u) qg[u] = __builtin_amdgcn_raw_buffer_load_b128(rs, qoff + u * 1024, 0, AUX_SC1);
    }
#pragma unroll
    for (int u = 0; u < 4; ++u) {
      v2u v; v[0] = qg[u][0]; v[1] = qg[u][2];
      *(LAS v2u*)(L + L_QS + u * 512 + lane * 8) = v;
    }
  }
  wg_barrier();
  STAMP(4);

  const v4u qb1 = *(const LAS v4u*)(L + L_QS + (c & 7) * 256 + (4 * g + (c >> 3)) * 16);
  const v4u qb2 = *(const LAS v4u*)(L + L_QS + (c & 7) * 256 + (4 * g + 2 + (c >> 3)) * 16);
  const bool lo8 = c < 8;
  float S[16];
  const f32x4 zero4 = f32x4{0.f, 0.f, 0.f, 0.f};
#pragma unroll
  for (int pr = 0; pr < 16; ++pr) {
    const f32x4 a1 = mfma(kv[2 * pr], qb1, zero4), a2 = mfma(kv[2 * pr], qb2, zero4);
    const f32x4 b1 = mfma(kv[2 * pr + 1], qb1, zero4), b2 = mfma(kv[2 * pr + 1], qb2, zero4);
    kv[2 * pr] = ldnt(vp + (2 * pr) * 1024);
    kv[2 * pr + 1] = ldnt(vp + (2 * pr + 1) * 1024);
    const float own = lo8 ? (a1[0] + a2[2]) : (b1[1] + b2[3]);
    const float oth = lo8 ? (b1[0] + b2[2]) : (a1[1] + a2[3]);
    S[pr] = own + dpp<DPP_ROR8>(oth);
    SCHED_FENCE();
  }
  STAMP(5);
  const int oh = (wg >> 2) & 31, ocb = wg & 3;
  {
    const GAS unsigned char* wp = wo + ((size_t)(oh * 128 + 32 * (w >> 1) + 8 * g) * DM + ocb * 256 + 128 * (w & 1) + 8 * c) * 2;
#pragma unroll
    for (int i = 0; i < 8; ++i) wreg[i] = ldnt(wp + (size_t)i * DM * 2);
  }
  SCHED_FENCE();

  constexpr float C2 = 0.08838834764831845f * 1.4426950408889634f;
  float m = S[0];
#pragma unroll
  for (int pr = 1; pr < 16; ++pr) m = fmaxf(m, S[pr]);
  m = fmaxf(m, dpp<DPP_ROR8>(m)); m = x16_max(m); m = x32_max(m);
  const float mc = m * C2;
  v4u pb[4];
#pragma unroll
  for (int pr = 0; pr < 16; ++pr) {
    const float pv = __builtin_amdgcn_exp2f(fmaf(S[pr], C2, -mc));
    pb[pr >> 2][pr & 3] = cvtpk(pv, dpp<DPP_ROR8>(pv));
  }
  f32x4 acc[8];
#pragma unroll
  for (int j = 0; j < 8; ++j) acc[j] = zero4;
  f32x4 lacc = zero4;
  const v4u ones = v4u{0x3f803f80u, 0x3f803f80u, 0x3f803f80u, 0x3f803f80u};
#pragma unroll
  for (int be = 0; be < 4; ++be) {
    const v4u (&blk)[8] = *(const v4u (*)[8])(&kv[8 * be]);
    tr_mfma8(blk, pb[be], acc);
    lacc = mfma(ones, pb[be], lacc);
  }
  STAMP(6);
  if (lo8) {
    LAS f32x4* op = (LAS f32x4*)(L + L_OPART + ((w * 8 + c) * OP_STR + 32 * g) * 4);
#pragma unroll
    for (int r = 0; r < 4; ++r) {
      op[2 * r] = f32x4{acc[0][r], acc[1][r], acc[2][r], acc[3][r]};
      op[2 * r + 1] = f32x4{acc[4][r], acc[5][r], acc[6][r], acc[7][r]};
    }
    if (g == 0) *(LAS f32x2*)(L + L_ML + (w * 8 + c) * 8) = f32x2{mc, lacc[0]};
  }
  wg_barrier();
  {
    float mw[8], lw[8], M = -INFINITY;
#pragma unroll
    for (int u = 0; u < 8; ++u) { const f32x2 v = *(const LAS f32x2*)(L + L_ML + (u * 8 + w) * 8); mw[u] = v[0]; lw[u] = v[1]; M = fmaxf(M, v[0]); }
    float Ls = 0.f, o0 = 0.f, o1 = 0.f;
#pragma unroll
    for (int u = 0; u < 8; ++u) {
      const float f = __builtin_amdgcn_exp2f(mw[u] - M);
      const f32x2 v = *(const LAS f32x2*)(L + L_OPART + ((u * 8 + w) * OP_STR + 2 * lane) * 4);
      Ls = fmaf(f, lw[u], Ls); o0 = fmaf(f, v[0], o0); o1 = fmaf(f, v[1], o1);
    }
    const float inv = 1.0f / Ls;
    v4u gr; gr[0] = asu(o0 * inv); gr[1] = tag; gr[2] = asu(o1 * inv); gr[3] = tag;
    __builtin_amdgcn_raw_buffer_store_b128(gr, rs, WS_PX + (wg * 8 + w) * 1024 + lane * 16, 0, AUX_SC1);
    if (lane == 0) {
      v2u lg; lg[0] = asu(M + __builtin_amdgcn_logf(Ls)); lg[1] = tag;
      __builtin_amdgcn_raw_buffer_store_b64(lg, rs, WS_LX + (((br * 32 + kvh * 8 + w) * 4 + b) * 8 + sp) * 8, 0, AUX_SC1);
    }
  }
  STAMP(7);
  {
    const int ob = w >> 1, hf = w & 1;
    const int pbase = WS_PX + ((((br * 4 + ob) * 4 + (oh >> 3)) * 8 + (lane >> 5)) * 8 + (oh & 7)) * 1024 + hf * 512 + (lane & 31) * 16;
    const int lbase = WS_LX + (((br * 32 + oh) * 4 + ob) * 8 + (lane & 7)) * 8;
    v4u pv[4]; v2u lg;
    for (unsigned spins = 0; spins < SPIN_CAP; ++spins) {
#pragma unroll
      for (int u = 0; u < 4; ++u) pv[u] = __builtin_amdgcn_raw_buffer_load_b128(rs, pbase + u * 2 * 8192, 0, AUX_SC1);
      lg = __builtin_amdgcn_raw_buffer_load_b64(rs, lbase, 0, AUX_SC1);
      bool ok = lg[1] == tag;
#pragma unroll
      for (int u = 0; u < 4; ++u) ok = ok && pv[u][1] == tag && pv[u][3] == tag;
      if (__all(ok)) break;
      __builtin_amdgcn_s_sleep(2);
    }
    const float lse = asf(lg[0]);
    float M = lse;
    M = fmaxf(M, dpp<DPP_XOR1>(M)); M = fmaxf(M, dpp<DPP_XOR2>(M)); M = fmaxf(M, dpp<0x141>(M));
    const float wt = __builtin_amdgcn_exp2f(lse - M);
    float W = wt;
    W += dpp<DPP_XOR1>(W); W += dpp<DPP_XOR2>(W); W += dpp<0x141>(W);
    const float wn = wt / W;
    float o0 = 0.f, o1 = 0.f;
#pragma unroll
    for (int u = 0; u < 4; ++u) {
      const float f = __shfl(wn, 2 * u + (lane >> 5), 64);
      o0 = fmaf(f, asf(pv[u][0]), o0); o1 = fmaf(f, asf(pv[u][2]), o1);
    }
    o0 = x32_sum(o0); o1 = x32_sum(o1);
    if (lane < 32) *(LAS unsigned*)(L + L_OB + ob * 256 + hf * 128 + lane * 4) = cvtpk(o0, o1);
  }
  wg_barrier();
  STAMP(8);
  {
    const v4u ob = *(const LAS v4u*)(L + L_OB + (c & 3) * 256 + (32 * (w >> 1) + 8 * g) * 2);
    f32x4 oa[8];
#pragma unroll
    for (int j = 0; j < 8; ++j) oa[j] = zero4;
    tr_mfma8(wreg, ob, oa);
    if (c < 4) {
      LAS f32x4* op = (LAS f32x4*)(L + L_WOP + (((w >> 1) * 4 + c) * 260 + 128 * (w & 1) + 32 * g) * 4);
#pragma unroll
      for (int r = 0; r < 4; ++r) {
        op[2 * r] = f32x4{oa[0][r], oa[1][r], oa[2][r], oa[3][r]};
        op[2 * r + 1] = f32x4{oa[4][r], oa[5][r], oa[6][r], oa[7][r]};
      }
    }
  }
  wg_barrier();
  {
    const int r = tid >> 3, ob = (tid >> 1) & 3, cp = tid & 1;
    float z = 0.f;
#pragma unroll
    for (int u = 0; u < 8; ++u) z += ((const LAS float*)(L + L_ZP))[u * 4 + ob];
    const float gate = 1.0f / (1.0f + __expf(-z));
    const float gs = br ? 1.0f - gate : gate;
    float s0 = 0.f, s1 = 0.f;
#pragma unroll
    for (int u = 0; u < 4; ++u) {
      const f32x2 v = *(const LAS f32x2*)(L + L_WOP + ((u * 4 + ob) * 260 + 4 * r + 2 * cp) * 4);
      s0 += v[0]; s1 += v[1];
    }
    v4u gr; gr[0] = asu(s0 * gs); gr[1] = tag; gr[2] = asu(s1 * gs); gr[3] = tag;
    __builtin_amdgcn_raw_buffer_store_b128(gr, rs, WS_OX + (ocb * 64 + br * 32 + oh) * 8192 + tid * 16, 0, AUX_SC1);
  }
  STAMP(9);
  if (w == 0) {
    const int r = br * 32 + oh;
    const int obase = WS_OX + (ocb * 64 + (lane >> 3)) * 8192 + r * 128 + (lane & 7) * 16;
    v4u pv[8];
    for (unsigned spins = 0; spins < SPIN_CAP; ++spins) {
#pragma unroll
      for (int u = 0; u < 8; ++u) pv[u] = __builtin_amdgcn_raw_buffer_load_b128(rs, obase + u * 8 * 8192, 0, AUX_SC1);
      bool ok = true;
#pragma unroll
      for (int u = 0; u < 8; ++u) ok = ok && pv[u][1] == tag && pv[u][3] == tag;
      if (__all(ok)) break;
      __builtin_amdgcn_s_sleep(2);
    }
    float s0 = 0.f, s1 = 0.f;
#pragma unroll
    for (int u = 0; u < 8; ++u) { s0 += asf(pv[u][0]); s1 += asf(pv[u][2]); }
    s0 += dpp<DPP_ROR8>(s0); s1 += dpp<DPP_ROR8>(s1);
    s0 = x16_sum(s0); s1 = x16_sum(s1);
    s0 = x32_sum(s0); s1 = x32_sum(s1);
    if (lane < 8) ((GAS unsigned*)p.out)[((lane >> 1) * DM + ocb * 256 + 4 * r + 2 * (lane & 1)) >> 1] = cvtpk(s0, s1);
    if (wg == 0 && lane == 0) __hip_atomic_store((gu32*)p.ws, tag, __ATOMIC_RELAXED, __HIP_MEMORY_SCOPE_AGENT);
  }
  STAMP(10);
  STAMP_FLUSH();
}

extern "C" void kernel_launch(void* const* d_in, const int* in_sizes, int n_in, void* d_out, int out_size, void* d_ws,
                              size_t ws_size, hipStream_t stream) {
  Params p;
  p.x = d_in[0];
  p.kc0 = d_in[1]; p.vc0 = d_in[2]; p.wq0 = d_in[3]; p.wo0 = d_in[4];
  p.kc1 = d_in[6]; p.vc1 = d_in[7]; p.wq1 = d_in[8]; p.wo1 = d_in[9];
  p.gw = d_in[10];
  p.out = d_out;
  p.ws = d_ws;
  hipLaunchKernelGGL(fused_kernel, dim3(NWG), dim3(NTHR), 0, stream, p);
}
```

```cpp
#include <hip/hip_runtime.h>
#include <hip/hip_bf16.h>
#include <stdint.h>

#define GAS __attribute__((address_space(1)))
#define LAS __attribute__((address_space(3)))
typedef unsigned v4u __attribute__((ext_vector_type(4)));
typedef unsigned v2u __attribute__((ext_vector_type(2)));
typedef float f32x4 __attribute__((ext_vector_type(4)));
typedef float f32x2 __attribute__((ext_vector_type(2)));
typedef __bf16 bf16x8 __attribute__((ext_vector_type(8)));
typedef GAS unsigned gu32;

#define STAMPS 0
#define WIN 8
#define TAU16 0
#define LEAD 4
#define HOP2_STAGGER 10
#define GATHER_AT 4

constexpr int DM = 1024, NQ = 4096, SK = 8192, DH = 128;
constexpr int NWG = 256, NTHR = 512;
constexpr int AUX_SC1 = 16;
constexpr unsigned SPIN_CAP = 1u << 20;

constexpr int WS_EPOCH = 0;
constexpr int WS_QX = 4096;
constexpr int WS_LX = WS_QX + 32 * 2 * 8192;
constexpr int WS_PX = WS_LX + 2 * 32 * 4 * 8 * 8;
constexpr int WS_OX = WS_PX + 256 * 8 * 512;
constexpr int WS_END = WS_OX + 4 * 64 * 8192;
constexpr int WS_STAMP = WS_END;

constexpr int L_QPART = 0;
constexpr int L_OPART = 0;
constexpr int OP_STR = 132;
constexpr int L_WOP = 0;
constexpr int L_ML = 8 * 8 * OP_STR * 4;
constexpr int L_QS = L_ML + 8 * 8 * 2 * 4;
constexpr int L_ZP = L_QS + 2048;
constexpr int L_GT = L_ZP + 128;
constexpr int L_QF = L_GT + 16;
constexpr int L_OB = L_QF + 32;
constexpr int L_END = L_OB + 1024;

struct Params {
  const void* x;
  const void* kc0; const void* vc0; const void* wq0; const void* wo0;
  const void* kc1; const void* vc1; const void* wq1; const void* wo1;
  const void* gw;
  void* out;
  void* ws;
};

constexpr int AUX_NT = 2;
__device__ __forceinline__ v4u ldnt(__amdgpu_buffer_rsrc_t r, int voff, int soff) { return __builtin_amdgcn_raw_buffer_load_b128(r, voff, soff, AUX_NT); }
__device__ __forceinline__ __amdgpu_buffer_rsrc_t mkrsrc(const void* base, size_t off, int bytes) {
  return __builtin_amdgcn_make_buffer_rsrc((void*)((const unsigned char*)base + off), 0, bytes, 0x00020000);
}
__device__ __forceinline__ v4u ldpl(const GAS unsigned char* p) { return *(const GAS v4u*)p; }
__device__ __forceinline__ f32x4 mfma(v4u a, v4u b, f32x4 c) {
  return __builtin_amdgcn_mfma_f32_16x16x32_bf16(__builtin_bit_cast(bf16x8, a), __builtin_bit_cast(bf16x8, b), c, 0, 0, 0);
}
__device__ __forceinline__ unsigned perm_lo(unsigned a, unsigned b) { return __builtin_amdgcn_perm(b, a, 0x05040100u); }
__device__ __forceinline__ unsigned perm_hi(unsigned a, unsigned b) { return __builtin_amdgcn_perm(b, a, 0x07060302u); }
__device__ __forceinline__ float asf(unsigned u) { return __builtin_bit_cast(float, u); }
__device__ __forceinline__ unsigned asu(float f) { return __builtin_bit_cast(unsigned, f); }
__device__ __forceinline__ unsigned cvtpk(float lo, float hi) {
  typedef __bf16 bf16x2 __attribute__((ext_vector_type(2)));
  bf16x2 r; r[0] = (__bf16)lo; r[1] = (__bf16)hi;
  return __builtin_bit_cast(unsigned, r);
}
__device__ __forceinline__ float rbf(float x) { return (float)(__bf16)x; }
template <int CTRL> __device__ __forceinline__ float dpp(float x) {
  return __builtin_bit_cast(float, __builtin_amdgcn_mov_dpp(__builtin_bit_cast(int, x), CTRL, 0xf, 0xf, true));
}
constexpr int DPP_XOR1 = 0xB1, DPP_XOR2 = 0x4E, DPP_ROR8 = 0x128, DPP_HMIR = 0x141;
__device__ __forceinline__ float x16_max(float x) {
  auto s = __builtin_amdgcn_permlane16_swap(asu(x), asu(x), false, false);
  return fmaxf(asf(s[0]), asf(s[1]));
}
__device__ __forceinline__ float x32_max(float x) {
  auto s = __builtin_amdgcn_permlane32_swap(asu(x), asu(x), false, false);
  return fmaxf(asf(s[0]), asf(s[1]));
}
__device__ __forceinline__ float x16_sum(float x) {
  auto s = __builtin_amdgcn_permlane16_swap(asu(x), asu(x), false, false);
  return asf(s[0]) + asf(s[1]);
}
__device__ __forceinline__ float x32_sum(float x) {
  auto s = __builtin_amdgcn_permlane32_swap(asu(x), asu(x), false, false);
  return asf(s[0]) + asf(s[1]);
}
__device__ __forceinline__ void wg_barrier() {
  asm volatile("s_waitcnt lgkmcnt(0)" ::: "memory");
  __builtin_amdgcn_s_barrier();
  asm volatile("" ::: "memory");
}
__device__ __forceinline__ void wait_until(unsigned due) {
  asm volatile("1:\n\ts_memrealtime vcc\n\ts_waitcnt lgkmcnt(0)\n\ts_sub_u32 vcc_lo, vcc_lo, %0\n\ts_cmp_lt_i32 vcc_lo, 0\n\ts_cbranch_scc0 2f\n\ts_sleep 1\n\ts_branch 1b\n2:"
               :: "s"(due) : "scc", "vcc", "memory");
}
#define LOCK 2
#define ADAPT16 0
#define AD_HI 40
#define AD_LO (-40)
#define AD_OFF 0
__device__ __forceinline__ int lag_of(unsigned due) {
  int d;
  asm volatile("s_memrealtime vcc\n\ts_waitcnt lgkmcnt(0)\n\ts_sub_u32 %0, vcc_lo, %1" : "=s"(d) : "s"(due) : "scc", "vcc", "memory");
  return d;
}
template <int W> __device__ __forceinline__ void wait_window(int lag) {
  asm volatile("s_cmp_gt_i32 %0, %1\n\ts_cbranch_scc1 1f\n\ts_cmp_lt_i32 %0, %2\n\ts_cbranch_scc1 2f\n\t"
               "s_waitcnt vmcnt(%3)\n\ts_branch 3f\n1:\n\ts_waitcnt vmcnt(%4)\n\ts_branch 3f\n2:\n\ts_waitcnt vmcnt(%5)\n3:"
               :: "s"(lag), "n"(AD_HI), "n"(AD_LO), "n"(W - 1), "n"(W), "n"(W - 2) : "scc", "memory");
}
#define PACE(n) do { if (LOCK && (n) % (LOCK ? LOCK : 1) == 0) __builtin_amdgcn_s_barrier(); \
    if (ADAPT16) { if ((n) % 4 == 0) lag = lag_of(t_start + AD_OFF + ((n) * ADAPT16 >> 4)); wait_window<WIN>(lag); } else WAIT_VM(WIN - 1); \
    if (TAU16 && ((n) & 1) == 0) wait_until(t_start + (((n) - LEAD) * TAU16 >> 4)); } while (0)
#define SCHED_FENCE() __builtin_amdgcn_sched_barrier(0)
#define WAIT_VM(n) do { asm volatile("s_waitcnt vmcnt(%0)" ::"n"(n) : "memory"); } while (0)

__device__ __forceinline__ void tr_mfma8(const v4u& r0, const v4u& r1, const v4u& r2, const v4u& r3, const v4u& r4, const v4u& r5,
                                         const v4u& r6, const v4u& r7, v4u bfrag, f32x4 (&acc)[8]) {
#pragma unroll
  for (int t = 0; t < 4; ++t) {
    v4u lo, hi;
    lo[0] = perm_lo(r0[t], r1[t]); hi[0] = perm_hi(r0[t], r1[t]);
    lo[1] = perm_lo(r2[t], r3[t]); hi[1] = perm_hi(r2[t], r3[t]);
    lo[2] = perm_lo(r4[t], r5[t]); hi[2] = perm_hi(r4[t], r5[t]);
    lo[3] = perm_lo(r6[t], r7[t]); hi[3] = perm_hi(r6[t], r7[t]);
    acc[2 * t] = mfma(lo, bfrag, acc[2 * t]);
    acc[2 * t + 1] = mfma(hi, bfrag, acc[2 * t + 1]);
  }
}
template <int T0, int T1>
__device__ __forceinline__ void tr_mfma_part(const v4u& r0, const v4u& r1, const v4u& r2, const v4u& r3, const v4u& r4, const v4u& r5,
                                             const v4u& r6, const v4u& r7, v4u bfrag, f32x4 (&acc)[8]) {
#pragma unroll
  for (int t = T0; t < T1; ++t) {
    v4u lo, hi;
    lo[0] = perm_lo(r0[t], r1[t]); hi[0] = perm_hi(r0[t], r1[t]);
    lo[1] = perm_lo(r2[t], r3[t]); hi[1] = perm_hi(r2[t], r3[t]);
    lo[2] = perm_lo(r4[t], r5[t]); hi[2] = perm_hi(r4[t], r5[t]);
    lo[3] = perm_lo(r6[t], r7[t]); hi[3] = perm_hi(r6[t], r7[t]);
    acc[2 * t] = mfma(lo, bfrag, acc[2 * t]);
    acc[2 * t + 1] = mfma(hi, bfrag, acc[2 * t + 1]);
  }
}
#define TR_PART(T0, T1, a, o, bf, acc) tr_mfma_part<T0, T1>((a)[(o)], (a)[(o) + 1], (a)[(o) + 2], (a)[(o) + 3], (a)[(o) + 4], (a)[(o) + 5], (a)[(o) + 6], (a)[(o) + 7], bf, acc)
#define TR_MFMA8(a, o, bf, acc) tr_mfma8((a)[(o)], (a)[(o) + 1], (a)[(o) + 2], (a)[(o) + 3], (a)[(o) + 4], (a)[(o) + 5], (a)[(o) + 6], (a)[(o) + 7], bf, acc)

__device__ __forceinline__ void poll_pair(__amdgpu_buffer_rsrc_t rs, int off, unsigned tag, float& v0, float& v1) {
  unsigned long long m; unsigned cnt;
  asm volatile(
      "buffer_load_dwordx4 v[248:251], %[off], %[rs], 0 offen sc1\n\t"
      "s_sleep %[st]\n\t"
      "s_mov_b32 %[cnt], 0\n"
      "1:\n\t"
      "buffer_load_dwordx4 v[252:255], %[off], %[rs], 0 offen sc1\n\t"
      "s_waitcnt vmcnt(1)\n\t"
      "v_cmp_eq_u32 vcc, %[tag], v249\n\t"
      "v_cmp_eq_u32 %[m], %[tag], v251\n\t"
      "s_and_b64 vcc, vcc, %[m]\n\t"
      "s_cmp_eq_u64 vcc, exec\n\t"
      "s_cbranch_scc1 2f\n\t"
      "buffer_load_dwordx4 v[248:251], %[off], %[rs], 0 offen sc1\n\t"
      "s_waitcnt vmcnt(1)\n\t"
      "v_cmp_eq_u32 vcc, %[tag], v253\n\t"
      "v_cmp_eq_u32 %[m], %[tag], v255\n\t"
      "s_and_b64 vcc, vcc, %[m]\n\t"
      "s_cmp_eq_u64 vcc, exec\n\t"
      "s_cbranch_scc1 3f\n\t"
      "s_add_u32 %[cnt], %[cnt], 1\n\t"
      "s_cmp_lt_u32 %[cnt], %[cap]\n\t"
      "s_cbranch_scc1 1b\n"
      "2:\n\t"
      "v_mov_b32 %[o0], v248\n\t"
      "v_mov_b32 %[o1], v250\n\t"
      "s_branch 4f\n"
      "3:\n\t"
      "v_mov_b32 %[o0], v252\n\t"
      "v_mov_b32 %[o1], v254\n"
      "4:"
      : [o0] "=&v"(v0), [o1] "=&v"(v1), [m] "=&s"(m), [cnt] "=&s"(cnt)
      : [off] "v"(off), [rs] "s"(rs), [tag] "s"(tag), [st] "i"(HOP2_STAGGER), [cap] "s"(SPIN_CAP)
      : "vcc", "scc", "memory", "v248", "v249", "v250", "v251", "v252", "v253", "v254", "v255");
}

#define STAMP(i) do { } while (0)
#define STAMP_FLUSH() do { } while (0)

__global__ void __launch_bounds__(NTHR, 2) fused_kernel(Params p) {
  __shared__ __attribute__((aligned(16))) unsigned char smem[L_END];
  LAS unsigned char* L = (LAS unsigned char*)smem;
  const int tid = threadIdx.x, lane = tid & 63;
  const int w = __builtin_amdgcn_readfirstlane(tid >> 6);
  const int wg = blockIdx.x;
  const int br = wg >> 7, b = (wg >> 5) & 3, kvh = (wg >> 3) & 3, sp = wg & 7;
  const int g = lane >> 4, c = lane & 15;
  const GAS unsigned char* wsb = (const GAS unsigned char*)p.ws;
  const __amdgpu_buffer_rsrc_t rs = __builtin_amdgcn_make_buffer_rsrc(p.ws, 0, WS_END, 0x00020000);
  STAMP(0);

  const unsigned t_start = (TAU16 + ADAPT16) ? (unsigned)__builtin_amdgcn_s_memrealtime() : 0u;
  int lag = 0;
  const unsigned epoch = *(const gu32*)p.ws;

  const GAS unsigned char* xq = (const GAS unsigned char*)p.x;
  const int qt = (sp + br * 4 + kvh) & 7;
  const int cj = 2 * qt + (b >> 1), kq = b & 1;
  const v4u xa = ldpl(xq + (((c >> 1) & 3) * DM + 512 * kq + 64 * w + 16 * g + 8 * (c & 1)) * 2);
  const int xk = 128 * w + 32 * g + 8 * (c & 3);
  const v4u xf = ldpl(xq + ((c >> 2) * DM + xk) * 2);
  const v4u gf = ldpl((const GAS unsigned char*)p.gw + xk * 2);
  const __amdgpu_buffer_rsrc_t rq = mkrsrc(br ? p.wq1 : p.wq0, 0, DM * NQ * 2);
  const int qvo = ((512 * kq + 64 * w + 8 * (lane >> 3)) * NQ + kvh * 1024 + cj * 64 + 8 * (lane & 7)) * 2;
  const size_t kvbase = ((size_t)(b * 4 + kvh) * SK + sp * 1024 + w * 16) * (DH * 2);
  const __amdgpu_buffer_rsrc_t rk = mkrsrc(br ? p.kc1 : p.kc0, kvbase, 7 * 32768 + 4096);
  const __amdgpu_buffer_rsrc_t rv = mkrsrc(br ? p.vc1 : p.vc0, kvbase, 7 * 32768 + 4096);
  const int kvo = (c >> 2) * 256 + g * 64 + (c & 3) * 16, vvo = lane * 16;
#define LD_KROW(i) ldnt(rk, kvo + ((i) & 3) * 1024, ((i) >> 2) * 32768)
#define LD_VROW(i) ldnt(rv, vvo + ((i) & 3) * 1024, ((i) >> 2) * 32768)
  const int oh = (wg & 7) | ((wg >> 2) & 24), ocb = (wg >> 3) & 3;
  v4u wreg[8], kv[32];
#define TOUCH 0
  unsigned touch = 0;
  if (TOUCH) {
    const int t_ox = WS_OX + (ocb * 64 + 8 * w + (lane >> 3)) * 8192 + (br * 32 + oh) * 128;
    const int t_px = WS_PX + (((br * 4 + (w >> 1)) * 4 + (oh >> 3)) * 8 + (lane & 7)) * 4096;
    const int t_own = lane < 32 ? WS_PX + wg * 4096 : lane < 48 ? WS_OX + (ocb * 64 + br * 32 + oh) * 8192 : WS_LX + (br * 32 + oh) * 256;
    touch = __builtin_amdgcn_raw_buffer_load_b32(rs, t_ox, 0, AUX_SC1) ^ __builtin_amdgcn_raw_buffer_load_b32(rs, t_px, 0, AUX_SC1) ^
            __builtin_amdgcn_raw_buffer_load_b32(rs, t_own, 0, AUX_SC1);
    const __amdgpu_buffer_rsrc_t rvt = mkrsrc(br ? p.vc1 : p.vc0, kvbase, 7 * 32768 + 4096);
    const __amdgpu_buffer_rsrc_t rot = mkrsrc(br ? p.wo1 : p.wo0, 0, NQ * DM * 2);
    touch ^= __builtin_amdgcn_raw_buffer_load_b32(rvt, (lane & 7) * 32768, 0, AUX_NT);
    touch ^= __builtin_amdgcn_raw_buffer_load_b32(rot, (oh * 128 + 16 * w + (lane & 15)) * DM * 2 + ocb * 512, 0, AUX_NT);
    touch ^= *((const gu32*)p.out + (lane & 3) * (DM / 2) + ocb * 128);
  }
#define WQ_EVERY 1
#define HEAD_LEN (WQ_EVERY ? 8 * WQ_EVERY : 12)
#define IS_WQ(n) (WQ_EVERY ? (n) % WQ_EVERY == 0 : (n) % 3 != 2)
#define WQ_IDX(n) (WQ_EVERY ? (n) / (WQ_EVERY ? WQ_EVERY : 1) : (n) - (n) / 3)
#define K_IDX(n) (WQ_EVERY ? (n) - (n) / (WQ_EVERY ? WQ_EVERY : 1) - 1 : (n) / 3)
#define WQ_LAST (WQ_EVERY ? 7 * WQ_EVERY : 10)
#define ISSUE_HEAD(n) do { if ((n) < HEAD_LEN) { if (IS_WQ(n)) wreg[WQ_IDX(n)] = ldnt(rq, qvo, WQ_IDX(n) * NQ * 2); else kv[K_IDX(n)] = LD_KROW(K_IDX(n)); } \
    else if ((n) < 40) kv[(n) - 8] = LD_KROW((n) - 8); else wreg[(n) - 40] = LD_VROW((n) - 40); SCHED_FENCE(); } while (0)
  if (tid < 8) ((LAS unsigned*)(L + L_QF))[tid] = 0u;
#pragma unroll
  for (int n = 0; n < WIN; ++n) ISSUE_HEAD(n);
  wg_barrier();
  constexpr int QP_AT = WQ_LAST + WIN;
#pragma unroll
  for (int n = WIN; n <= QP_AT; ++n) { PACE(n); ISSUE_HEAD(n); }

  const unsigned tag = __builtin_amdgcn_readfirstlane(epoch) + 1u;
  {
    f32x4 acc[8];
#pragma unroll
    for (int j = 0; j < 8; ++j) acc[j] = f32x4{0.f, 0.f, 0.f, 0.f};
    TR_MFMA8(wreg, 0, xa, acc);
    if (c < 8 && (c & 1) == (g >> 1)) {
      LAS f32x4* qp = (LAS f32x4*)(L + L_QPART) + ((w * 2 + (g >> 1)) * 4 + (c >> 1)) * 16 + (g & 1) * 8;
#pragma unroll
      for (int r = 0; r < 4; ++r) {
        qp[2 * r] = f32x4{acc[0][r], acc[1][r], acc[2][r], acc[3][r]};
        qp[2 * r + 1] = f32x4{acc[4][r], acc[5][r], acc[6][r], acc[7][r]};
      }
    }
    float z = 0.f;
#pragma unroll
    for (int t = 0; t < 4; ++t) {
      z += rbf(asf(xf[t] << 16) * asf(gf[t] << 16));
      z += rbf(asf(xf[t] & 0xffff0000u) * asf(gf[t] & 0xffff0000u));
    }
    z += dpp<DPP_XOR1>(z); z += dpp<DPP_XOR2>(z); z = x16_sum(z); z = x32_sum(z);
    if (g == 0 && (c & 3) == 0) ((LAS float*)(L + L_ZP))[w * 4 + (c >> 2)] = z;
    asm volatile("s_waitcnt lgkmcnt(0)" ::: "memory");
    unsigned old = 0;
    if (lane == 0) old = __hip_atomic_fetch_add((LAS unsigned*)(L + L_QF + 16), 1u, __ATOMIC_RELAXED, __HIP_MEMORY_SCOPE_WORKGROUP);
    STAMP(1);
    asm volatile("" :: "v"(touch));
    if (__builtin_amdgcn_readfirstlane(old) == 7u) {
      asm volatile("" ::: "memory");
#pragma unroll
      for (int h = 0; h < 2; ++h) {
        const int t = lane + 64 * h;
        const LAS f32x2* qp = (const LAS f32x2*)(L + L_QPART) + t;
        float s0 = 0.f, s1 = 0.f;
#pragma unroll
        for (int u = 0; u < 16; ++u) { const f32x2 v = qp[u * 128]; s0 += v[0]; s1 += v[1]; }
        v4u gr; gr[0] = asu(s0); gr[1] = tag; gr[2] = asu(s1); gr[3] = tag;
        __builtin_amdgcn_raw_buffer_store_b128(gr, rs, WS_QX + ((((br * 4 + kvh) * 4 + (t >> 5)) * 2 + kq) * 1024 + cj * 64 + 2 * (t & 31)) * 8, 0, AUX_SC1);
      }
      if (lane < 4) {
        float zz = 0.f;
#pragma unroll
        for (int u = 0; u < 8; ++u) zz += ((const LAS float*)(L + L_ZP))[u * 4 + lane];
        ((LAS float*)(L + L_GT))[lane] = rbf(1.0f / (1.0f + __expf(-rbf(zz))));
      }
    }
  }
  SCHED_FENCE();
  STAMP(2);
  v4u qg[4];
  bool ok = w >= 4;
  const int qoff = WS_QX + (((br * 4 + kvh) * 4 + b) * 2 * 1024 + (w & 3) * 256 + 2 * lane) * 8;
#define Q_LOADS() do { asm volatile("" ::: "memory"); _Pragma("unroll") for (int u_ = 0; u_ < 4; ++u_) qg[u_] = __builtin_amdgcn_raw_buffer_load_b128(rs, qoff + (u_ & 1) * 8192 + (u_ >> 1) * 1024, 0, AUX_SC1); } while (0)
#define Q_TEST() do { bool t_ = true; _Pragma("unroll") for (int u_ = 0; u_ < 4; ++u_) t_ = t_ & (qg[u_][1] == tag) & (qg[u_][3] == tag); \
      const bool a_ = __all(t_); \
      if (!ok && a_) { ok = true; \
        *(LAS unsigned*)(L + L_QS + w * 512 + lane * 4) = cvtpk(asf(qg[0][0]) + asf(qg[1][0]), asf(qg[0][2]) + asf(qg[1][2])); \
        *(LAS unsigned*)(L + L_QS + w * 512 + 256 + lane * 4) = cvtpk(asf(qg[2][0]) + asf(qg[3][0]), asf(qg[2][2]) + asf(qg[3][2])); \
        asm volatile("" ::: "memory"); \
        if (lane == 0) *(volatile LAS unsigned*)(L + L_QF + w * 4) = 1u; } } while (0)
#define Q_POLL() do { if (!ok) Q_LOADS(); SCHED_FENCE(); } while (0)
#define Q_CHECK() do { if (w < 4) Q_TEST(); SCHED_FENCE(); } while (0)
#define POLL0 (QP_AT + 6)
#pragma unroll
  for (int n = QP_AT + 1; n < 48; ++n) {
    PACE(n);
    if (n == POLL0 || n == POLL0 + 8 || (n == POLL0 + 16 && POLL0 + 16 < 48)) Q_POLL();
    ISSUE_HEAD(n);
    if (n == POLL0 + 7 || n == POLL0 + 15 || n == POLL0 + 23) Q_CHECK();
  }
  STAMP(3);
  if (POLL0 + 23 >= 48 || POLL0 + 15 >= 48) Q_CHECK();
  if (w < 4) for (unsigned spins = 0; !ok && spins < SPIN_CAP; ++spins) { Q_LOADS(); Q_TEST(); }
#pragma unroll 1
  for (unsigned spins = 0; spins < SPIN_CAP; ++spins) {
    const v4u fl = *(volatile LAS v4u*)(L + L_QF);
    if (__builtin_amdgcn_readfirstlane(fl[0] & fl[1] & fl[2] & fl[3])) break;
    __builtin_amdgcn_s_sleep(1);
  }
  asm volatile("" ::: "memory");
  STAMP(4);

  const v4u qb1 = *(const LAS v4u*)(L + L_QS + (c & 7) * 256 + (4 * g + (c >> 3)) * 16);
  const v4u qb2 = *(const LAS v4u*)(L + L_QS + (c & 7) * 256 + (4 * g + 2 + (c >> 3)) * 16);
  const bool lo8 = c < 8;
  float S[16];
  const f32x4 zero4 = f32x4{0.f, 0.f, 0.f, 0.f};
  v4u vb[24];
#pragma unroll
  for (int pr = 0; pr < 16; ++pr) {
    PACE(48 + pr);
    vb[pr] = LD_VROW(8 + pr);
    SCHED_FENCE();
    const f32x4 a1 = mfma(kv[2 * pr], qb1, zero4), a2 = mfma(kv[2 * pr], qb2, zero4);
    const f32x4 b1 = mfma(kv[2 * pr + 1], qb1, zero4), b2 = mfma(kv[2 * pr + 1], qb2, zero4);
    const float own = lo8 ? (a1[0] + a2[2]) : (b1[1] + b2[3]);
    const float oth = lo8 ? (b1[0] + b2[2]) : (a1[1] + a2[3]);
    S[pr] = rbf(rbf(own + dpp<DPP_ROR8>(oth)) * 0.08838834764831845f);
    SCHED_FENCE();
  }
  STAMP(5);
  const __amdgpu_buffer_rsrc_t ro = mkrsrc(br ? p.wo1 : p.wo0, 0, NQ * DM * 2);
  const int ovo = ((oh * 128 + 32 * (w >> 1) + 2 * g) * DM + ocb * 256 + 128 * (w & 1) + 8 * c) * 2;
  v4u wt[8];
#define LD_V(i) do { SCHED_FENCE(); PACE(40 + (i)); vb[(i) - 8] = LD_VROW(i); SCHED_FENCE(); } while (0)
#define LD_WO(i) do { SCHED_FENCE(); PACE(72 + (i)); wt[i] = ldnt(ro, ovo, (8 * ((i) & 3) + ((i) >> 2)) * DM * 2); SCHED_FENCE(); } while (0)
  constexpr float LOG2E = 1.4426950408889634f;
  LD_V(24);
  float m = S[0];
#pragma unroll
  for (int pr = 1; pr < 16; ++pr) m = fmaxf(m, S[pr]);
  m = fmaxf(m, dpp<DPP_ROR8>(m)); m = x16_max(m); m = x32_max(m);
  v4u pb[4];
  LD_V(25);
#pragma unroll
  for (int pr = 0; pr < 16; ++pr) {
    const float pv = __builtin_amdgcn_exp2f((S[pr] - m) * LOG2E);
    pb[pr >> 2][pr & 3] = cvtpk(pv, dpp<DPP_ROR8>(pv));
  }
  f32x4 lacc = zero4;
  const v4u ones = v4u{0x3f803f80u, 0x3f803f80u, 0x3f803f80u, 0x3f803f80u};
#pragma unroll
  for (int be = 0; be < 4; ++be) lacc = mfma(ones, pb[be], lacc);
  if (lo8 && g == 0) *(LAS f32x2*)(L + L_ML + (w * 8 + c) * 8) = f32x2{m * LOG2E, lacc[0]};
  f32x4 acc[8];
#pragma unroll
  for (int j = 0; j < 8; ++j) acc[j] = zero4;
  LD_V(26);
  TR_PART(0, 2, wreg, 0, pb[0], acc);
  LD_V(27);
  TR_PART(2, 4, wreg, 0, pb[0], acc);
  wg_barrier();
  float msc[8], lse;
  {
    float mw[8], lw[8], M = -INFINITY, Ls = 0.f;
#pragma unroll
    for (int u = 0; u < 8; ++u) { const f32x2 v = *(const LAS f32x2*)(L + L_ML + (u * 8 + w) * 8); mw[u] = v[0]; lw[u] = v[1]; M = fmaxf(M, v[0]); }
#pragma unroll
    for (int u = 0; u < 8; ++u) { msc[u] = __builtin_amdgcn_exp2f(mw[u] - M); Ls = fmaf(msc[u], lw[u], Ls); }
    const float inv = __builtin_amdgcn_rcpf(Ls);
#pragma unroll
    for (int u = 0; u < 8; ++u) msc[u] *= inv;
    lse = M + __builtin_amdgcn_logf(Ls);
  }
  LD_V(28);
  TR_PART(0, 2, vb, 0, pb[1], acc);
  LD_V(29);
  TR_PART(2, 4, vb, 0, pb[1], acc);
  LD_V(30);
  LD_V(31);
  TR_PART(0, 2, vb, 8, pb[2], acc);
  LD_WO(0);
  TR_PART(2, 4, vb, 8, pb[2], acc);
  LD_WO(1); LD_WO(2); LD_WO(3); LD_WO(4); LD_WO(5); LD_WO(6); LD_WO(7);
  TR_MFMA8(vb, 16, pb[3], acc);
  STAMP(6);
  if (lo8) {
    LAS f32x4* op = (LAS f32x4*)(L + L_OPART + ((w * 8 + c) * OP_STR + 32 * g) * 4);
#pragma unroll
    for (int j = 0; j < 8; ++j) op[j] = acc[j];
  }
  wg_barrier();
  {
    float o0 = 0.f, o1 = 0.f;
#pragma unroll
    for (int u = 0; u < 8; ++u) {
      const f32x2 v = *(const LAS f32x2*)(L + L_OPART + ((u * 8 + w) * OP_STR + 2 * lane) * 4);
      o0 = fmaf(msc[u], v[0], o0); o1 = fmaf(msc[u], v[1], o1);
    }
    v2u gr; gr[0] = cvtpk(o0, o1); gr[1] = tag;
    __builtin_amdgcn_raw_buffer_store_b64(gr, rs, WS_PX + (wg * 8 + w) * 512 + lane * 8, 0, AUX_SC1);
    if (lane == 0) {
      v2u lg; lg[0] = asu(lse); lg[1] = tag;
      __builtin_amdgcn_raw_buffer_store_b64(lg, rs, WS_LX + (((br * 32 + kvh * 8 + w) * 4 + b) * 8 + sp) * 8, 0, AUX_SC1);
    }
  }
  STAMP(7);
  {
    const int ob = w >> 1, hf = w & 1;
    const int pbase = WS_PX + ((((br * 4 + ob) * 4 + (oh >> 3)) * 8 + g) * 8 + (oh & 7)) * 512 + hf * 256 + c * 16;
    const int lbase = WS_LX + (((br * 32 + oh) * 4 + ob) * 8 + g) * 8;
    v4u pv0, pv1; v2u lg0, lg1;
    bool lane_ok = false;
    WAIT_VM(GATHER_AT + 2);
    for (unsigned spins = 0; spins < SPIN_CAP; ++spins) {
      asm volatile("" ::: "memory");
      if (!lane_ok) {
        pv0 = __builtin_amdgcn_raw_buffer_load_b128(rs, pbase, 0, AUX_SC1);
        pv1 = __builtin_amdgcn_raw_buffer_load_b128(rs, pbase + 4 * 4096, 0, AUX_SC1);
        lg0 = __builtin_amdgcn_raw_buffer_load_b64(rs, lbase, 0, AUX_SC1);
        lg1 = __builtin_amdgcn_raw_buffer_load_b64(rs, lbase + 32, 0, AUX_SC1);
      }
      lane_ok = (lg0[1] == tag) & (lg1[1] == tag) & (pv0[1] == tag) & (pv0[3] == tag) & (pv1[1] == tag) & (pv1[3] == tag);
      unsigned long long miss = __ballot(!lane_ok);
      if (miss == 0ull) break;
      while (__builtin_popcountll(miss) > 16 && ++spins < SPIN_CAP) {
        __builtin_amdgcn_s_sleep(2);
        asm volatile("" ::: "memory");
        const v2u t0 = __builtin_amdgcn_raw_buffer_load_b64(rs, lbase, 0, AUX_SC1);
        const v2u t1 = __builtin_amdgcn_raw_buffer_load_b64(rs, lbase + 32, 0, AUX_SC1);
        miss = __ballot(!((t0[1] == tag) & (t1[1] == tag)));
      }
    }
    STAMP(8);
    const float l0 = asf(lg0[0]), l1 = asf(lg1[0]);
    const float M = x32_max(x16_max(fmaxf(l0, l1)));
    const float w0 = __builtin_amdgcn_exp2f(l0 - M), w1 = __builtin_amdgcn_exp2f(l1 - M);
    const float inv = __builtin_amdgcn_rcpf(x32_sum(x16_sum(w0 + w1)));
    const float f0 = w0 * inv, f1 = w1 * inv;
    float o[4];
    o[0] = f0 * asf(pv0[0] << 16) + f1 * asf(pv1[0] << 16);
    o[1] = f0 * asf(pv0[0] & 0xffff0000u) + f1 * asf(pv1[0] & 0xffff0000u);
    o[2] = f0 * asf(pv0[2] << 16) + f1 * asf(pv1[2] << 16);
    o[3] = f0 * asf(pv0[2] & 0xffff0000u) + f1 * asf(pv1[2] & 0xffff0000u);
#pragma unroll
    for (int e = 0; e < 4; ++e) o[e] = x32_sum(x16_sum(o[e]));
    if (lane < 16) *(LAS v2u*)(L + L_OB + ob * 256 + hf * 128 + lane * 8) = v2u{cvtpk(o[0], o[1]), cvtpk(o[2], o[3])};
  }
  wg_barrier();
  {
    const v4u ob = *(const LAS v4u*)(L + L_OB + (c & 3) * 256 + (32 * (w >> 1) + 8 * g) * 2);
    f32x4 oa[8];
#pragma unroll
    for (int j = 0; j < 8; ++j) oa[j] = zero4;
    TR_MFMA8(wt, 0, ob, oa);
    if (c < 4) {
      LAS f32x4* op = (LAS f32x4*)(L + L_WOP + (((w >> 1) * 4 + c) * 260 + 128 * (w & 1) + 32 * g) * 4);
#pragma unroll
      for (int j = 0; j < 8; ++j) op[j] = oa[j];
    }
  }
  wg_barrier();
  STAMP(9);
  {
    const int r = tid >> 3, ob = (tid >> 1) & 3, cp = tid & 1;
    const int col = 4 * r + 2 * cp;
    const int pos = (col & ~31) + 4 * (col & 7) + ((col >> 3) & 3);
    float s0 = 0.f, s1 = 0.f;
#pragma unroll
    for (int u = 0; u < 4; ++u) {
      const LAS float* q = (const LAS float*)(L + L_WOP) + (u * 4 + ob) * 260 + pos;
      s0 += q[0]; s1 += q[4];
    }
    v4u gr; gr[0] = asu(s0); gr[1] = tag; gr[2] = asu(s1); gr[3] = tag;
    __builtin_amdgcn_raw_buffer_store_b128(gr, rs, WS_OX + (ocb * 64 + br * 32 + oh) * 8192 + tid * 16, 0, AUX_SC1);
  }
  STAMP(10);
  {
    const int r = br * 32 + oh;
    const int obase = WS_OX + (ocb * 64 + 8 * w + (lane >> 3)) * 8192 + r * 128 + (lane & 7) * 16;
    float s0, s1;
#define HOP2_MODE 1
    if (HOP2_MODE == 0) poll_pair(rs, obase, tag, s0, s1);
    else {
      if (HOP2_MODE == 2) WAIT_VM(0);
      v4u pa;
      for (unsigned spins = 0; spins < SPIN_CAP; ++spins) {
        asm volatile("" ::: "memory");
        pa = __builtin_amdgcn_raw_buffer_load_b128(rs, obase, 0, AUX_SC1);
        if (__all((pa[1] == tag) & (pa[3] == tag))) break;
        if (HOP2_MODE == 3) __builtin_amdgcn_s_sleep(8);
      }
      s0 = asf(pa[0]); s1 = asf(pa[2]);
    }
    s0 += dpp<DPP_ROR8>(s0); s1 += dpp<DPP_ROR8>(s1);
    s0 = x32_sum(x16_sum(s0)); s1 = x32_sum(x16_sum(s1));
    if (lane < 8) *(LAS f32x2*)(L + L_ML + (w * 8 + lane) * 8) = f32x2{s0, s1};
    asm volatile("s_waitcnt lgkmcnt(0)" ::: "memory");
    unsigned old = 0;
    if (lane == 0) old = __hip_atomic_fetch_add((LAS unsigned*)(L + L_QF + 20), 1u, __ATOMIC_RELAXED, __HIP_MEMORY_SCOPE_WORKGROUP);
    if (__builtin_amdgcn_readfirstlane(old) == 7u) {
      asm volatile("" ::: "memory");
      if (lane < 8) {
        float a0 = 0.f, a1 = 0.f, b0 = 0.f, b1 = 0.f;
#pragma unroll
        for (int u = 0; u < 4; ++u) {
          const f32x2 va = *(const LAS f32x2*)(L + L_ML + (u * 8 + lane) * 8), vb2 = *(const LAS f32x2*)(L + L_ML + ((u + 4) * 8 + lane) * 8);
          a0 += va[0]; a1 += va[1]; b0 += vb2[0]; b1 += vb2[1];
        }
        const float gate = ((const LAS float*)(L + L_GT))[lane >> 1];
        const float og = rbf(1.0f - gate);
        float ta0 = rbf(gate * rbf(a0)), ta1 = rbf(gate * rbf(a1)), tb0 = rbf(og * rbf(b0)), tb1 = rbf(og * rbf(b1));
        asm volatile("" : "+v"(ta0), "+v"(ta1), "+v"(tb0), "+v"(tb1));
        ((GAS unsigned*)p.out)[((lane >> 1) * DM + ocb * 256 + 4 * r + 2 * (lane & 1)) >> 1] = cvtpk(ta0 + tb0, ta1 + tb1);
        if (wg == 0 && lane == 0) __hip_atomic_store((gu32*)p.ws, tag, __ATOMIC_RELAXED, __HIP_MEMORY_SCOPE_AGENT);
      }
    }
  }
  STAMP(11);
  STAMP_FLUSH();
}

extern "C" void kernel_launch(void* const* d_in, const int* in_sizes, int n_in, void* d_out, int out_size, void* d_ws,
                              size_t ws_size, hipStream_t stream) {
  Params p;
  p.x = d_in[0];
  p.kc0 = d_in[1]; p.vc0 = d_in[2]; p.wq0 = d_in[3]; p.wo0 = d_in[4];
  p.kc1 = d_in[6]; p.vc1 = d_in[7]; p.wq1 = d_in[8]; p.wo1 = d_in[9];
  p.gw = d_in[10];
  p.out = d_out;
  p.ws = d_ws;
  hipLaunchKernelGGL(fused_kernel, dim3(NWG), dim3(NTHR), 0, stream, p);
}
```

```cpp
#include <hip/hip_runtime.h>
#include <hip/hip_bf16.h>
#include <stdint.h>

#define GAS __attribute__((address_space(1)))
#define LAS __attribute__((address_space(3)))
typedef unsigned v4u __attribute__((ext_vector_type(4)));
typedef unsigned v2u __attribute__((ext_vector_type(2)));
typedef float f32x4 __attribute__((ext_vector_type(4)));
typedef float f32x2 __attribute__((ext_vector_type(2)));
typedef __bf16 bf16x8 __attribute__((ext_vector_type(8)));
typedef GAS unsigned gu32;

#define STAMPS 0
#define WIN 8
#define TAU16 464
#define LEAD 4
#define HOP2_STAGGER 10
#define GATE_TICKS 0
#define PRESTORE 0
#define OXSWZ 1
#define PRE_MASK 3
#define T_EARLY 0
#define GATHER_AT 4

constexpr int DM = 1024, NQ = 4096, SK = 8192, DH = 128;
constexpr int NWG = 256, NTHR = 512;
constexpr int AUX_SC1 = 16;
constexpr unsigned SPIN_CAP = 1u << 20;

constexpr int WS_EPOCH = 0;
constexpr int WS_QX = 4096;
constexpr int PX_REC = 640;
constexpr int WS_PX = WS_QX + 32 * 2 * 8192;
constexpr int WS_OX = WS_PX + 256 * 8 * PX_REC;
constexpr int WS_END = WS_OX + 4 * 64 * 8192;
constexpr int WS_STAMP = WS_END;

constexpr int L_QPART = 0;
constexpr int L_OPART = 0;
constexpr int OP_STR = 132;
constexpr int L_WOP = 0;
constexpr int L_ML = 8 * 8 * OP_STR * 4;
constexpr int L_QS = L_ML + 8 * 8 * 2 * 4;
constexpr int L_ZP = L_QS + 2048;
constexpr int L_GT = L_ZP + 128;
constexpr int L_QF = L_GT + 16;
constexpr int L_OB = L_QF + 32;
constexpr int L_END = L_OB + 1024;

struct Params {
  const void* x;
  const void* kc0; const void* vc0; const void* wq0; const void* wo0;
  const void* kc1; const void* vc1; const void* wq1; const void* wo1;
  const void* gw;
  void* out;
  void* ws;
};

constexpr int AUX_NT = 2;
__device__ __forceinline__ v4u ldnt(__amdgpu_buffer_rsrc_t r, int voff, int soff) { return __builtin_amdgcn_raw_buffer_load_b128(r, voff, soff, AUX_NT); }
__device__ __forceinline__ __amdgpu_buffer_rsrc_t mkrsrc(const void* base, size_t off, int bytes) {
  return __builtin_amdgcn_make_buffer_rsrc((void*)((const unsigned char*)base + off), 0, bytes, 0x00020000);
}
__device__ __forceinline__ v4u ldpl(const GAS unsigned char* p) { return *(const GAS v4u*)p; }
__device__ __forceinline__ f32x4 mfma(v4u a, v4u b, f32x4 c) {
  return __builtin_amdgcn_mfma_f32_16x16x32_bf16(__builtin_bit_cast(bf16x8, a), __builtin_bit_cast(bf16x8, b), c, 0, 0, 0);
}
__device__ __forceinline__ unsigned perm_lo(unsigned a, unsigned b) { return __builtin_amdgcn_perm(b, a, 0x05040100u); }
__device__ __forceinline__ unsigned perm_hi(unsigned a, unsigned b) { return __builtin_amdgcn_perm(b, a, 0x07060302u); }
__device__ __forceinline__ float asf(unsigned u) { return __builtin_bit_cast(float, u); }
__device__ __forceinline__ unsigned asu(float f) { return __builtin_bit_cast(unsigned, f); }
__device__ __forceinline__ unsigned cvtpk(float lo, float hi) {
  typedef __bf16 bf16x2 __attribute__((ext_vector_type(2)));
  bf16x2 r; r[0] = (__bf16)lo; r[1] = (__bf16)hi;
  return __builtin_bit_cast(unsigned, r);
}
__device__ __forceinline__ float rbf(float x) { return (float)(__bf16)x; }
template <int CTRL> __device__ __forceinline__ float dpp(float x) {
  return __builtin_bit_cast(float, __builtin_amdgcn_mov_dpp(__builtin_bit_cast(int, x), CTRL, 0xf, 0xf, true));
}
constexpr int DPP_XOR1 = 0xB1, DPP_XOR2 = 0x4E, DPP_ROR8 = 0x128, DPP_HMIR = 0x141;
__device__ __forceinline__ float x16_max(float x) {
  auto s = __builtin_amdgcn_permlane16_swap(asu(x), asu(x), false, false);
  return fmaxf(asf(s[0]), asf(s[1]));
}
__device__ __forceinline__ float x32_max(float x) {
  auto s = __builtin_amdgcn_permlane32_swap(asu(x), asu(x), false, false);
  return fmaxf(asf(s[0]), asf(s[1]));
}
__device__ __forceinline__ float x16_sum(float x) {
  auto s = __builtin_amdgcn_permlane16_swap(asu(x), asu(x), false, false);
  return asf(s[0]) + asf(s[1]);
}
__device__ __forceinline__ float x32_sum(float x) {
  auto s = __builtin_amdgcn_permlane32_swap(asu(x), asu(x), false, false);
  return asf(s[0]) + asf(s[1]);
}
__device__ __forceinline__ void wg_barrier() {
  asm volatile("s_waitcnt lgkmcnt(0)" ::: "memory");
  __builtin_amdgcn_s_barrier();
  asm volatile("" ::: "memory");
}
__device__ __forceinline__ void wait_until(unsigned due) {
  asm volatile("1:\n\ts_memrealtime vcc\n\ts_waitcnt lgkmcnt(0)\n\ts_sub_u32 vcc_lo, vcc_lo, %0\n\ts_cmp_lt_i32 vcc_lo, 0\n\ts_cbranch_scc0 2f\n\ts_sleep 1\n\ts_branch 1b\n2:"
               :: "s"(due) : "scc", "vcc", "memory");
}
#define LOCK 2
#define ADAPT16 0
#define AD_HI 40
#define AD_LO (-40)
#define AD_UP 1
#define AD_DN 1
#define AD_OFF 0
__device__ __forceinline__ int lag_of(unsigned due) {
  int d;
  asm volatile("s_memrealtime vcc\n\ts_waitcnt lgkmcnt(0)\n\ts_sub_u32 %0, vcc_lo, %1" : "=s"(d) : "s"(due) : "scc", "vcc", "memory");
  return d;
}
template <int W> __device__ __forceinline__ void wait_window(int lag) {
  asm volatile("s_cmp_gt_i32 %0, %1\n\ts_cbranch_scc1 1f\n\ts_cmp_lt_i32 %0, %2\n\ts_cbranch_scc1 2f\n\t"
               "s_waitcnt vmcnt(%3)\n\ts_branch 3f\n1:\n\ts_waitcnt vmcnt(%4)\n\ts_branch 3f\n2:\n\ts_waitcnt vmcnt(%5)\n3:"
               :: "s"(lag), "n"(AD_HI), "n"(AD_LO), "n"(W - 1), "n"(W - 1 + AD_UP), "n"(W - 1 - AD_DN) : "scc", "memory");
}
#define PACE(n) do { if (LOCK && (n) % (LOCK ? LOCK : 1) == 0) __builtin_amdgcn_s_barrier(); \
    if (ADAPT16) { if ((n) % 4 == 0) lag = lag_of(t_start + AD_OFF + ((n) * ADAPT16 >> 4)); wait_window<WIN>(lag); } else WAIT_VM(WIN - 1); \
    if (TAU16 && ((n) & 1) == 0) wait_until(t_start + (((n) - LEAD) * TAU16 >> 4)); } while (0)
#define SCHED_FENCE() __builtin_amdgcn_sched_barrier(0)
#define WAIT_VM(n) do { asm volatile("s_waitcnt vmcnt(%0)" ::"n"(n) : "memory"); } while (0)

__device__ __forceinline__ void tr_mfma8(const v4u& r0, const v4u& r1, const v4u& r2, const v4u& r3, const v4u& r4, const v4u& r5,
                                         const v4u& r6, const v4u& r7, v4u bfrag, f32x4 (&acc)[8]) {
#pragma unroll
  for (int t = 0; t < 4; ++t) {
    v4u lo, hi;
    lo[0] = perm_lo(r0[t], r1[t]); hi[0] = perm_hi(r0[t], r1[t]);
    lo[1] = perm_lo(r2[t], r3[t]); hi[1] = perm_hi(r2[t], r3[t]);
    lo[2] = perm_lo(r4[t], r5[t]); hi[2] = perm_hi(r4[t], r5[t]);
    lo[3] = perm_lo(r6[t], r7[t]); hi[3] = perm_hi(r6[t], r7[t]);
    acc[2 * t] = mfma(lo, bfrag, acc[2 * t]);
    acc[2 * t + 1] = mfma(hi, bfrag, acc[2 * t + 1]);
  }
}
template <int T0, int T1>
__device__ __forceinline__ void tr_mfma_part(const v4u& r0, const v4u& r1, const v4u& r2, const v4u& r3, const v4u& r4, const v4u& r5,
                                             const v4u& r6, const v4u& r7, v4u bfrag, f32x4 (&acc)[8]) {
#pragma unroll
  for (int t = T0; t < T1; ++t) {
    v4u lo, hi;
    lo[0] = perm_lo(r0[t], r1[t]); hi[0] = perm_hi(r0[t], r1[t]);
    lo[1] = perm_lo(r2[t], r3[t]); hi[1] = perm_hi(r2[t], r3[t]);
    lo[2] = perm_lo(r4[t], r5[t]); hi[2] = perm_hi(r4[t], r5[t]);
    lo[3] = perm_lo(r6[t], r7[t]); hi[3] = perm_hi(r6[t], r7[t]);
    acc[2 * t] = mfma(lo, bfrag, acc[2 * t]);
    acc[2 * t + 1] = mfma(hi, bfrag, acc[2 * t + 1]);
  }
}
#define TR_PART(T0, T1, a, o, bf, acc) tr_mfma_part<T0, T1>((a)[(o)], (a)[(o) + 1], (a)[(o) + 2], (a)[(o) + 3], (a)[(o) + 4], (a)[(o) + 5], (a)[(o) + 6], (a)[(o) + 7], bf, acc)
#define TR_MFMA8(a, o, bf, acc) tr_mfma8((a)[(o)], (a)[(o) + 1], (a)[(o) + 2], (a)[(o) + 3], (a)[(o) + 4], (a)[(o) + 5], (a)[(o) + 6], (a)[(o) + 7], bf, acc)

__device__ __forceinline__ void poll_pair(__amdgpu_buffer_rsrc_t rs, int off, unsigned tag, float& v0, float& v1) {
  unsigned long long m; unsigned cnt;
  asm volatile(
      "buffer_load_dwordx4 v[248:251], %[off], %[rs], 0 offen sc1\n\t"
      "s_sleep %[st]\n\t"
      "s_mov_b32 %[cnt], 0\n"
      "1:\n\t"
      "buffer_load_dwordx4 v[252:255], %[off], %[rs], 0 offen sc1\n\t"
      "s_waitcnt vmcnt(1)\n\t"
      "v_cmp_eq_u32 vcc, %[tag], v249\n\t"
      "v_cmp_eq_u32 %[m], %[tag], v251\n\t"
      "s_and_b64 vcc, vcc, %[m]\n\t"
      "s_cmp_eq_u64 vcc, exec\n\t"
      "s_cbranch_scc1 2f\n\t"
      "buffer_load_dwordx4 v[248:251], %[off], %[rs], 0 offen sc1\n\t"
      "s_waitcnt vmcnt(1)\n\t"
      "v_cmp_eq_u32 vcc, %[tag], v253\n\t"
      "v_cmp_eq_u32 %[m], %[tag], v255\n\t"
      "s_and_b64 vcc, vcc, %[m]\n\t"
      "s_cmp_eq_u64 vcc, exec\n\t"
      "s_cbranch_scc1 3f\n\t"
      "s_add_u32 %[cnt], %[cnt], 1\n\t"
      "s_cmp_lt_u32 %[cnt], %[cap]\n\t"
      "s_cbranch_scc1 1b\n"
      "2:\n\t"
      "v_mov_b32 %[o0], v248\n\t"
      "v_mov_b32 %[o1], v250\n\t"
      "s_branch 4f\n"
      "3:\n\t"
      "v_mov_b32 %[o0], v252\n\t"
      "v_mov_b32 %[o1], v254\n"
      "4:"
      : [o0] "=&v"(v0), [o1] "=&v"(v1), [m] "=&s"(m), [cnt] "=&s"(cnt)
      : [off] "v"(off), [rs] "s"(rs), [tag] "s"(tag), [st] "i"(HOP2_STAGGER), [cap] "s"(SPIN_CAP)
      : "vcc", "scc", "memory", "v248", "v249", "v250", "v251", "v252", "v253", "v254", "v255");
}

#define STAMP(i) do { } while (0)
#define STAMP_FLUSH() do { } while (0)

__global__ void __launch_bounds__(NTHR, 2) fused_kernel(Params p) {
  __shared__ __attribute__((aligned(16))) unsigned char smem[L_END];
  LAS unsigned char* L = (LAS unsigned char*)smem;
  const int tid = threadIdx.x, lane = tid & 63;
  const int w = __builtin_amdgcn_readfirstlane(tid >> 6);
  const int wg = blockIdx.x;
  const int br = wg >> 7, b = (wg >> 5) & 3, kvh = (wg >> 3) & 3, sp = wg & 7;
  const int g = lane >> 4, c = lane & 15;
  const GAS unsigned char* wsb = (const GAS unsigned char*)p.ws;
  const __amdgpu_buffer_rsrc_t rs = __builtin_amdgcn_make_buffer_rsrc(p.ws, 0, WS_END, 0x00020000);
  STAMP(0);

  const unsigned t_start = (TAU16 + ADAPT16 + GATE_TICKS + T_EARLY) ? (unsigned)__builtin_amdgcn_s_memrealtime() : 0u;
  int lag = 0;
  const unsigned epoch = *(const gu32*)p.ws;

  const GAS unsigned char* xq = (const GAS unsigned char*)p.x;
#define QMAP 1
  const int qidx = (((sp >> 1) + kvh + 2 * br) & 3) * 4 + b;
  const int cj = QMAP ? (int)((((sp & 1) ? 0xD5C49180u : 0xF7E6A2B3u) >> (4 * (qidx >> 1))) & 15u) : 2 * ((sp + br * 4 + kvh) & 7) + (b >> 1);
  const int kq = b & 1;
  const v4u xa = ldpl(xq + (((c >> 1) & 3) * DM + 512 * kq + 64 * w + 16 * g + 8 * (c & 1)) * 2);
  const int xk = 128 * w + 32 * g + 8 * (c & 3);
  const v4u xf = ldpl(xq + ((c >> 2) * DM + xk) * 2);
  const v4u gf = ldpl((const GAS unsigned char*)p.gw + xk * 2);
  const __amdgpu_buffer_rsrc_t rq = mkrsrc(br ? p.wq1 : p.wq0, 0, DM * NQ * 2);
  const int qvo = ((512 * kq + 64 * w + 8 * (lane >> 3)) * NQ + kvh * 1024 + cj * 64 + 8 * (lane & 7)) * 2;
  const size_t kvbase = ((size_t)(b * 4 + kvh) * SK + sp * 1024 + w * 16) * (DH * 2);
  const __amdgpu_buffer_rsrc_t rk = mkrsrc(br ? p.kc1 : p.kc0, kvbase, 7 * 32768 + 4096);
  const __amdgpu_buffer_rsrc_t rv = mkrsrc(br ? p.vc1 : p.vc0, kvbase, 7 * 32768 + 4096);
  const int kvo = (c >> 2) * 256 + g * 64 + (c & 3) * 16, vvo = lane * 16;
#define LD_KROW(i) ldnt(rk, kvo + ((i) & 3) * 1024, ((i) >> 2) * 32768)
#define LD_VROW(i) ldnt(rv, vvo + ((i) & 3) * 1024, ((i) >> 2) * 32768)
  const int oh = (wg & 7) | ((wg >> 2) & 24), ocb = (wg >> 3) & 3;
  v4u wreg[8], kv[32];
#define WQ_EVERY 1
#define HEAD_LEN (WQ_EVERY ? 8 * WQ_EVERY : 12)
#define IS_WQ(n) (WQ_EVERY ? (n) % WQ_EVERY == 0 : (n) % 3 != 2)
#define WQ_IDX(n) (WQ_EVERY ? (n) / (WQ_EVERY ? WQ_EVERY : 1) : (n) - (n) / 3)
#define K_IDX(n) (WQ_EVERY ? (n) - (n) / (WQ_EVERY ? WQ_EVERY : 1) - 1 : (n) / 3)
#define WQ_LAST (WQ_EVERY ? 7 * WQ_EVERY : 10)
#define ISSUE_HEAD(n) do { if ((n) < HEAD_LEN) { if (IS_WQ(n)) wreg[WQ_IDX(n)] = ldnt(rq, qvo, WQ_IDX(n) * NQ * 2); else kv[K_IDX(n)] = LD_KROW(K_IDX(n)); } \
    else if ((n) < 40) kv[(n) - 8] = LD_KROW((n) - 8); else wreg[(n) - 40] = LD_VROW((n) - 40); SCHED_FENCE(); } while (0)
  if (tid < 8) ((LAS unsigned*)(L + L_QF))[tid] = 0u;
#pragma unroll
  for (int n = 0; n < WIN; ++n) ISSUE_HEAD(n);
  wg_barrier();
  constexpr int QP_AT = WQ_LAST + WIN;
#pragma unroll
  for (int n = WIN; n <= QP_AT; ++n) { PACE(n); ISSUE_HEAD(n); }

  const unsigned tag = __builtin_amdgcn_readfirstlane(epoch) + 1u;
  {
    f32x4 acc[8];
#pragma unroll
    for (int j = 0; j < 8; ++j) acc[j] = f32x4{0.f, 0.f, 0.f, 0.f};
    TR_MFMA8(wreg, 0, xa, acc);
    if (c < 8 && (c & 1) == (g >> 1)) {
      LAS f32x4* qp = (LAS f32x4*)(L + L_QPART) + ((w * 2 + (g >> 1)) * 4 + (c >> 1)) * 16 + (g & 1) * 8;
#pragma unroll
      for (int r = 0; r < 4; ++r) {
        qp[2 * r] = f32x4{acc[0][r], acc[1][r], acc[2][r], acc[3][r]};
        qp[2 * r + 1] = f32x4{acc[4][r], acc[5][r], acc[6][r], acc[7][r]};
      }
    }
    float z = 0.f;
#pragma unroll
    for (int t = 0; t < 4; ++t) {
      z += rbf(asf(xf[t] << 16) * asf(gf[t] << 16));
      z += rbf(asf(xf[t] & 0xffff0000u) * asf(gf[t] & 0xffff0000u));
    }
    z += dpp<DPP_XOR1>(z); z += dpp<DPP_XOR2>(z); z = x16_sum(z); z = x32_sum(z);
    if (g == 0 && (c & 3) == 0) ((LAS float*)(L + L_ZP))[w * 4 + (c >> 2)] = z;
    asm volatile("s_waitcnt lgkmcnt(0)" ::: "memory");
    unsigned old = 0;
    if (lane == 0) old = __hip_atomic_fetch_add((LAS unsigned*)(L + L_QF + 16), 1u, __ATOMIC_RELAXED, __HIP_MEMORY_SCOPE_WORKGROUP);
    STAMP(1);
    if (__builtin_amdgcn_readfirstlane(old) == 7u) {
      asm volatile("" ::: "memory");
#pragma unroll
      for (int h = 0; h < 2; ++h) {
        const int t = lane + 64 * h;
        const LAS f32x2* qp = (const LAS f32x2*)(L + L_QPART) + t;
        float s0 = 0.f, s1 = 0.f;
#pragma unroll
        for (int u = 0; u < 16; ++u) { const f32x2 v = qp[u * 128]; s0 += v[0]; s1 += v[1]; }
        v4u gr; gr[0] = asu(s0); gr[1] = tag; gr[2] = asu(s1); gr[3] = tag;
        __builtin_amdgcn_raw_buffer_store_b128(gr, rs, WS_QX + ((((br * 4 + kvh) * 4 + (t >> 5)) * 2 + kq) * 1024 + cj * 64 + 2 * (t & 31)) * 8, 0, AUX_SC1);
      }
      if (lane < 4) {
        float zz = 0.f;
#pragma unroll
        for (int u = 0; u < 8; ++u) zz += ((const LAS float*)(L + L_ZP))[u * 4 + lane];
        ((LAS float*)(L + L_GT))[lane] = rbf(1.0f / (1.0f + __expf(-rbf(zz))));
      }
    }
  }
  SCHED_FENCE();
  STAMP(2);
  v4u qg[4];
  bool ok = w >= 4;
  const int qoff = WS_QX + (((br * 4 + kvh) * 4 + b) * 2 * 1024 + (w & 3) * 256 + 2 * lane) * 8;
#define Q_LOADS() do { asm volatile("" ::: "memory"); _Pragma("unroll") for (int u_ = 0; u_ < 4; ++u_) qg[u_] = __builtin_amdgcn_raw_buffer_load_b128(rs, qoff + (u_ & 1) * 8192 + (u_ >> 1) * 1024, 0, AUX_SC1); } while (0)
#define Q_TEST() do { bool t_ = true; _Pragma("unroll") for (int u_ = 0; u_ < 4; ++u_) t_ = t_ & (qg[u_][1] == tag) & (qg[u_][3] == tag); \
      const bool a_ = __all(t_); \
      if (!ok && a_) { ok = true; \
        *(LAS unsigned*)(L + L_QS + w * 512 + lane * 4) = cvtpk(asf(qg[0][0]) + asf(qg[1][0]), asf(qg[0][2]) + asf(qg[1][2])); \
        *(LAS unsigned*)(L + L_QS + w * 512 + 256 + lane * 4) = cvtpk(asf(qg[2][0]) + asf(qg[3][0]), asf(qg[2][2]) + asf(qg[3][2])); \
        asm volatile("" ::: "memory"); \
        if (lane == 0) *(volatile LAS unsigned*)(L + L_QF + w * 4) = 1u; } } while (0)
#define Q_POLL() do { if (!ok) Q_LOADS(); SCHED_FENCE(); } while (0)
#define Q_CHECK() do { if (w < 4) Q_TEST(); SCHED_FENCE(); } while (0)
#define POLL0 (QP_AT + 6)
#pragma unroll
  for (int n = QP_AT + 1; n < 48; ++n) {
    PACE(n);
    if (n == POLL0 || n == POLL0 + 8 || (n == POLL0 + 16 && POLL0 + 16 < 48)) Q_POLL();
    ISSUE_HEAD(n);
    if (PRESTORE && n == PRESTORE) {
      const v4u z = v4u{0u, tag - 1u, 0u, tag - 1u};
      if (PRE_MASK & 1) __builtin_amdgcn_raw_buffer_store_b128(z, rs, WS_OX + (ocb * 64 + br * 32 + oh) * 8192 + tid * 16, 0, AUX_SC1);
      if ((PRE_MASK & 2) && lane < 40) __builtin_amdgcn_raw_buffer_store_b128(z, rs, WS_PX + (wg * 8 + w) * PX_REC + lane * 16, 0, AUX_SC1);
      SCHED_FENCE();
    }
    if (n == POLL0 + 7 || n == POLL0 + 15 || n == POLL0 + 23) Q_CHECK();
  }
  STAMP(3);
  if (POLL0 + 23 >= 48 || POLL0 + 15 >= 48) Q_CHECK();
  if (w < 4) for (unsigned spins = 0; !ok && spins < SPIN_CAP; ++spins) { Q_LOADS(); Q_TEST(); }
#pragma unroll 1
  for (unsigned spins = 0; spins < SPIN_CAP; ++spins) {
    const v4u fl = *(volatile LAS v4u*)(L + L_QF);
    if (__builtin_amdgcn_readfirstlane(fl[0] & fl[1] & fl[2] & fl[3])) break;
    __builtin_amdgcn_s_sleep(1);
  }
  asm volatile("" ::: "memory");
  STAMP(4);

  const v4u qb1 = *(const LAS v4u*)(L + L_QS + (c & 7) * 256 + (4 * g + (c >> 3)) * 16);
  const v4u qb2 = *(const LAS v4u*)(L + L_QS + (c & 7) * 256 + (4 * g + 2 + (c >> 3)) * 16);
  const bool lo8 = c < 8;
  float S[16];
  const f32x4 zero4 = f32x4{0.f, 0.f, 0.f, 0.f};
  v4u vb[24];
#pragma unroll
  for (int pr = 0; pr < 16; ++pr) {
    PACE(48 + pr);
    vb[pr] = LD_VROW(8 + pr);
    SCHED_FENCE();
    const f32x4 a1 = mfma(kv[2 * pr], qb1, zero4), a2 = mfma(kv[2 * pr], qb2, zero4);
    const f32x4 b1 = mfma(kv[2 * pr + 1], qb1, zero4), b2 = mfma(kv[2 * pr + 1], qb2, zero4);
    const float own = lo8 ? (a1[0] + a2[2]) : (b1[1] + b2[3]);
    const float oth = lo8 ? (b1[0] + b2[2]) : (a1[1] + a2[3]);
    S[pr] = rbf(rbf(own + dpp<DPP_ROR8>(oth)) * 0.08838834764831845f);
    SCHED_FENCE();
  }
  STAMP(5);
  const __amdgpu_buffer_rsrc_t ro = mkrsrc(br ? p.wo1 : p.wo0, 0, NQ * DM * 2);
  const int ovo = ((oh * 128 + 32 * (w >> 1) + 2 * g) * DM + ocb * 256 + 128 * (w & 1) + 8 * c) * 2;
  v4u wt[8];
#define LD_V(i) do { SCHED_FENCE(); PACE(40 + (i)); vb[(i) - 8] = LD_VROW(i); SCHED_FENCE(); } while (0)
#define LD_WO(i) do { SCHED_FENCE(); PACE(72 + (i)); wt[i] = ldnt(ro, ovo, (8 * ((i) & 3) + ((i) >> 2)) * DM * 2); SCHED_FENCE(); } while (0)
  constexpr float LOG2E = 1.4426950408889634f;
  LD_V(24);
  float m = S[0];
#pragma unroll
  for (int pr = 1; pr < 16; ++pr) m = fmaxf(m, S[pr]);
  m = fmaxf(m, dpp<DPP_ROR8>(m)); m = x16_max(m); m = x32_max(m);
  v4u pb[4];
  LD_V(25);
#pragma unroll
  for (int pr = 0; pr < 16; ++pr) {
    const float pv = __builtin_amdgcn_exp2f((S[pr] - m) * LOG2E), px = dpp<DPP_ROR8>(pv);
    pb[pr >> 2][pr & 3] = cvtpk(lo8 ? pv : px, lo8 ? px : pv);
  }
  f32x4 lacc = zero4;
  const v4u ones = v4u{0x3f803f80u, 0x3f803f80u, 0x3f803f80u, 0x3f803f80u};
#pragma unroll
  for (int be = 0; be < 4; ++be) lacc = mfma(ones, pb[be], lacc);
  if (lo8 && g == 0) *(LAS f32x2*)(L + L_ML + (w * 8 + c) * 8) = f32x2{m * LOG2E, lacc[0]};
  f32x4 acc[8];
#pragma unroll
  for (int j = 0; j < 8; ++j) acc[j] = zero4;
  LD_V(26);
  TR_PART(0, 2, wreg, 0, pb[0], acc);
  LD_V(27);
  TR_PART(2, 4, wreg, 0, pb[0], acc);
  wg_barrier();
  float msc[8], lse;
  {
    float mw[8], lw[8], M = -INFINITY, Ls = 0.f;
#pragma unroll
    for (int u = 0; u < 8; ++u) { const f32x2 v = *(const LAS f32x2*)(L + L_ML + (u * 8 + w) * 8); mw[u] = v[0]; lw[u] = v[1]; M = fmaxf(M, v[0]); }
#pragma unroll
    for (int u = 0; u < 8; ++u) { msc[u] = __builtin_amdgcn_exp2f(mw[u] - M); Ls = fmaf(msc[u], lw[u], Ls); }
    const float inv = __builtin_amdgcn_rcpf(Ls);
#pragma unroll
    for (int u = 0; u < 8; ++u) msc[u] *= inv;
    lse = M + __builtin_amdgcn_logf(Ls);
  }
  LD_V(28);
  TR_PART(0, 2, vb, 0, pb[1], acc);
  LD_V(29);
  TR_PART(2, 4, vb, 0, pb[1], acc);
  LD_V(30);
  LD_V(31);
  TR_PART(0, 2, vb, 8, pb[2], acc);
  LD_WO(0);
  TR_PART(2, 4, vb, 8, pb[2], acc);
  LD_WO(1); LD_WO(2); LD_WO(3); LD_WO(4); LD_WO(5); LD_WO(6); LD_WO(7);
  TR_MFMA8(vb, 16, pb[3], acc);
  STAMP(6);
  {
    LAS f32x4* op = (LAS f32x4*)(L + L_OPART + ((w * 8 + (c & 7)) * OP_STR + 32 * g) * 4) + (lo8 ? 0 : 4);
#pragma unroll
    for (int j = 0; j < 4; ++j) {
      f32x4 v;
#pragma unroll
      for (int e = 0; e < 4; ++e) v[e] = lo8 ? acc[j][e] : acc[j + 4][e];
      op[j] = v;
    }
  }
  wg_barrier();
  {
    float o0 = 0.f, o1 = 0.f;
#pragma unroll
    for (int u = 0; u < 8; ++u) {
      const f32x2 v = *(const LAS f32x2*)(L + L_OPART + ((u * 8 + w) * OP_STR + 2 * lane) * 4);
      o0 = fmaf(msc[u], v[0], o0); o1 = fmaf(msc[u], v[1], o1);
    }
    v2u gr; gr[0] = cvtpk(o0, o1); gr[1] = tag;
    __builtin_amdgcn_raw_buffer_store_b64(gr, rs, WS_PX + (wg * 8 + w) * PX_REC + lane * 8, 0, AUX_SC1);
    if (lane < 16) {
      v2u lg; lg[0] = asu(lse); lg[1] = tag;
      __builtin_amdgcn_raw_buffer_store_b64(lg, rs, WS_PX + (wg * 8 + w) * PX_REC + 512 + lane * 8, 0, AUX_SC1);
    }
  }
  STAMP(7);
  const int obase = WS_OX + (ocb * 64 + 8 * w + (lane >> 3)) * 8192 + ((br * 32 + oh + (OXSWZ ? 8 * w + (lane >> 3) : 0)) & 63) * 128 + (lane & 7) * 16;
  unsigned early = 0;
  if (T_EARLY && lag_of(t_start + T_EARLY) < 0) early = __builtin_amdgcn_raw_buffer_load_b32(rs, obase, 0, AUX_SC1);
  {
    const int ob = w >> 1, hf = w & 1;
    const int prec = WS_PX + ((((br * 4 + ob) * 4 + (oh >> 3)) * 8 + g) * 8 + (oh & 7)) * PX_REC;
    const int pbase = prec + hf * 256 + c * 16;
    const int lbase = prec + 512;
    v4u pv0, pv1; v2u lg0, lg1;
    bool lane_ok = false;
    WAIT_VM(GATHER_AT + 2);
#define H1_SLEEP 2
    if (GATE_TICKS) wait_until(t_start + GATE_TICKS);
    for (unsigned spins = 0; spins < SPIN_CAP; ++spins) {
      asm volatile("" ::: "memory");
      if (!lane_ok) {
        pv0 = __builtin_amdgcn_raw_buffer_load_b128(rs, pbase, 0, AUX_SC1);
        pv1 = __builtin_amdgcn_raw_buffer_load_b128(rs, pbase + 4 * 8 * PX_REC, 0, AUX_SC1);
        lg0 = __builtin_amdgcn_raw_buffer_load_b64(rs, lbase, 0, AUX_SC1);
        lg1 = __builtin_amdgcn_raw_buffer_load_b64(rs, lbase + 4 * 8 * PX_REC, 0, AUX_SC1);
      }
      lane_ok = (lg0[1] == tag) & (lg1[1] == tag) & (pv0[1] == tag) & (pv0[3] == tag) & (pv1[1] == tag) & (pv1[3] == tag);
      unsigned long long miss = __ballot(!lane_ok);
      if (miss == 0ull) break;
      while (__builtin_popcountll(miss) > 16 && ++spins < SPIN_CAP) {
        __builtin_amdgcn_s_sleep(H1_SLEEP);
        asm volatile("" ::: "memory");
        const v2u t0 = __builtin_amdgcn_raw_buffer_load_b64(rs, lbase, 0, AUX_SC1);
        const v2u t1 = __builtin_amdgcn_raw_buffer_load_b64(rs, lbase + 4 * 8 * PX_REC, 0, AUX_SC1);
        miss = __ballot(!((t0[1] == tag) & (t1[1] == tag)));
      }
    }
    STAMP(8);
    const float l0 = asf(lg0[0]), l1 = asf(lg1[0]);
    const float M = x32_max(x16_max(fmaxf(l0, l1)));
    const float w0 = __builtin_amdgcn_exp2f(l0 - M), w1 = __builtin_amdgcn_exp2f(l1 - M);
    const float inv = __builtin_amdgcn_rcpf(x32_sum(x16_sum(w0 + w1)));
    const float f0 = w0 * inv, f1 = w1 * inv;
    float o[4];
    o[0] = f0 * asf(pv0[0] << 16) + f1 * asf(pv1[0] << 16);
    o[1] = f0 * asf(pv0[0] & 0xffff0000u) + f1 * asf(pv1[0] & 0xffff0000u);
    o[2] = f0 * asf(pv0[2] << 16) + f1 * asf(pv1[2] << 16);
    o[3] = f0 * asf(pv0[2] & 0xffff0000u) + f1 * asf(pv1[2] & 0xffff0000u);
#pragma unroll
    for (int e = 0; e < 4; ++e) o[e] = x32_sum(x16_sum(o[e]));
    if (lane < 16) *(LAS v2u*)(L + L_OB + ob * 256 + hf * 128 + lane * 8) = v2u{cvtpk(o[0], o[1]), cvtpk(o[2], o[3])};
  }
  wg_barrier();
  {
    const v4u ob = *(const LAS v4u*)(L + L_OB + (c & 3) * 256 + (32 * (w >> 1) + 8 * g) * 2);
    f32x4 oa[8];
#pragma unroll
    for (int j = 0; j < 8; ++j) oa[j] = zero4;
    TR_MFMA8(wt, 0, ob, oa);
    {
      const int q = c >> 2;
      LAS f32x4* op = (LAS f32x4*)(L + L_WOP + (((w >> 1) * 4 + (c & 3)) * 260 + 128 * (w & 1) + 32 * g) * 4) + 2 * q;
      f32x4 x[4];
#pragma unroll
      for (int k = 0; k < 4; ++k)
#pragma unroll
        for (int e = 0; e < 4; ++e) x[k][e] = q < 2 ? oa[k][e] : oa[k + 4][e];
#pragma unroll
      for (int k = 0; k < 2; ++k) {
        f32x4 y;
#pragma unroll
        for (int e = 0; e < 4; ++e) y[e] = (q & 1) ? x[k + 2][e] : x[k][e];
        op[k] = y;
      }
    }
  }
  wg_barrier();
  STAMP(9);
  {
    const int r = tid >> 3, ob = (tid >> 1) & 3, cp = tid & 1;
    const int col = 4 * r + 2 * cp;
    const int pos = (col & ~31) + 4 * (col & 7) + ((col >> 3) & 3);
    float s0 = 0.f, s1 = 0.f;
#pragma unroll
    for (int u = 0; u < 4; ++u) {
      const LAS float* q = (const LAS float*)(L + L_WOP) + (u * 4 + ob) * 260 + pos;
      s0 += q[0]; s1 += q[4];
    }
    v4u gr; gr[0] = asu(s0); gr[1] = tag; gr[2] = asu(s1); gr[3] = tag;
    __builtin_amdgcn_raw_buffer_store_b128(gr, rs, WS_OX + (ocb * 64 + br * 32 + oh) * 8192 + ((r + (OXSWZ ? br * 32 + oh : 0)) & 63) * 128 + (tid & 7) * 16, 0, AUX_SC1);
  }
  STAMP(10);
  {
    const int r = br * 32 + oh;
    asm volatile("" :: "v"(early));
    float s0, s1;
#define HOP2_MODE 2
    if (HOP2_MODE == 0) poll_pair(rs, obase, tag, s0, s1);
    else {
      if (HOP2_MODE == 2) WAIT_VM(0);
      v4u pa;
      for (unsigned spins = 0; spins < SPIN_CAP; ++spins) {
        asm volatile("" ::: "memory");
        pa = __builtin_amdgcn_raw_buffer_load_b128(rs, obase, 0, AUX_SC1);
        if (__all((pa[1] == tag) & (pa[3] == tag))) break;
        if (HOP2_MODE == 3) __builtin_amdgcn_s_sleep(8);
      }
      s0 = asf(pa[0]); s1 = asf(pa[2]);
    }
    s0 += dpp<DPP_ROR8>(s0); s1 += dpp<DPP_ROR8>(s1);
    s0 = x32_sum(x16_sum(s0)); s1 = x32_sum(x16_sum(s1));
    if (lane < 8) *(LAS f32x2*)(L + L_ML + (w * 8 + lane) * 8) = f32x2{s0, s1};
    asm volatile("s_waitcnt lgkmcnt(0)" ::: "memory");
    unsigned old = 0;
    if (lane == 0) old = __hip_atomic_fetch_add((LAS unsigned*)(L + L_QF + 20), 1u, __ATOMIC_RELAXED, __HIP_MEMORY_SCOPE_WORKGROUP);
    if (__builtin_amdgcn_readfirstlane(old) == 7u) {
      asm volatile("" ::: "memory");
      if (lane < 8) {
        float a0 = 0.f, a1 = 0.f, b0 = 0.f, b1 = 0.f;
#pragma unroll
        for (int u = 0; u < 4; ++u) {
          const f32x2 va = *(const LAS f32x2*)(L + L_ML + (u * 8 + lane) * 8), vb2 = *(const LAS f32x2*)(L + L_ML + ((u + 4) * 8 + lane) * 8);
          a0 += va[0]; a1 += va[1]; b0 += vb2[0]; b1 += vb2[1];
        }
        const float gate = ((const LAS float*)(L + L_GT))[lane >> 1];
        const float og = rbf(1.0f - gate);
        float ta0 = rbf(gate * rbf(a0)), ta1 = rbf(gate * rbf(a1)), tb0 = rbf(og * rbf(b0)), tb1 = rbf(og * rbf(b1));
        asm volatile("" : "+v"(ta0), "+v"(ta1), "+v"(tb0), "+v"(tb1));
        ((GAS unsigned*)p.out)[((lane >> 1) * DM + ocb * 256 + 4 * r + 2 * (lane & 1)) >> 1] = cvtpk(ta0 + tb0, ta1 + tb1);
        if (wg == 0 && lane == 0) __hip_atomic_store((gu32*)p.ws, tag, __ATOMIC_RELAXED, __HIP_MEMORY_SCOPE_AGENT);
      }
    }
  }
  STAMP(11);
  STAMP_FLUSH();
}

extern "C" void kernel_launch(void* const* d_in, const int* in_sizes, int n_in, void* d_out, int out_size, void* d_ws,
                              size_t ws_size, hipStream_t stream) {
  Params p;
  p.x = d_in[0];
  p.kc0 = d_in[1]; p.vc0 = d_in[2]; p.wq0 = d_in[3]; p.wo0 = d_in[4];
  p.kc1 = d_in[6]; p.vc1 = d_in[7]; p.wq1 = d_in[8]; p.wo1 = d_in[9];
  p.gw = d_in[10];
  p.out = d_out;
  p.ws = d_ws;
  hipLaunchKernelGGL(fused_kernel, dim3(NWG), dim3(NTHR), 0, stream, p);
}
```

```cpp
#include <hip/hip_runtime.h>
#include <hip/hip_bf16.h>
#include <stdint.h>

#define GAS __attribute__((address_space(1)))
#define LAS __attribute__((address_space(3)))
typedef unsigned v4u __attribute__((ext_vector_type(4)));
typedef unsigned v2u __attribute__((ext_vector_type(2)));
typedef float f32x4 __attribute__((ext_vector_type(4)));
typedef float f32x2 __attribute__((ext_vector_type(2)));
typedef __bf16 bf16x8 __attribute__((ext_vector_type(8)));
typedef GAS unsigned gu32;

constexpr int WIN = 8;
constexpr int LOCK = 2;
constexpr int TAU16 = 512;
constexpr int LEAD = 6;
constexpr int POLL0 = 21;
constexpr int GATHER_AT = 4;

constexpr int DM = 1024, NQ = 4096, SK = 8192, DH = 128;
constexpr int NWG = 256, NTHR = 512;
constexpr int AUX_SC1 = 16;
constexpr unsigned SPIN_CAP = 1u << 17;

constexpr int WS_QX = 4096;
constexpr int PX_REC = 640;
constexpr int WS_PX = WS_QX + 32 * 2 * 8192;
constexpr int WS_OX = WS_PX + 256 * 8 * PX_REC;
constexpr int WS_END = WS_OX + 4 * 64 * 4096;

constexpr int L_QPART = 0;
constexpr int L_OPART = 0;
constexpr int OP_STR = 132;
constexpr int L_WOP = 0;
constexpr int L_ML = 8 * 8 * OP_STR * 4;
constexpr int L_QS = L_ML + 8 * 8 * 2 * 4;
constexpr int L_ZP = L_QS + 2048;
constexpr int L_GT = L_ZP + 128;
constexpr int L_QF = L_GT + 16;
constexpr int L_OB = L_QF + 32;
constexpr int L_END = L_OB + 1024;

struct Params {
  const void* x;
  const void* kc0; const void* vc0; const void* wq0; const void* wo0;
  const void* kc1; const void* vc1; const void* wq1; const void* wo1;
  const void* gw;
  void* out;
  void* ws;
};

constexpr int AUX_NT = 2;
__device__ __forceinline__ v4u ldnt(__amdgpu_buffer_rsrc_t r, int voff, int soff) { return __builtin_amdgcn_raw_buffer_load_b128(r, voff, soff, AUX_NT); }
__device__ __forceinline__ __amdgpu_buffer_rsrc_t mkrsrc(const void* base, size_t off, int bytes) {
  return __builtin_amdgcn_make_buffer_rsrc((void*)((const unsigned char*)base + off), 0, bytes, 0x00020000);
}
__device__ __forceinline__ v4u ldpl(const GAS unsigned char* p) { return *(const GAS v4u*)p; }
__device__ __forceinline__ f32x4 mfma(v4u a, v4u b, f32x4 c) {
  return __builtin_amdgcn_mfma_f32_16x16x32_bf16(__builtin_bit_cast(bf16x8, a), __builtin_bit_cast(bf16x8, b), c, 0, 0, 0);
}
__device__ __forceinline__ unsigned perm_lo(unsigned a, unsigned b) { return __builtin_amdgcn_perm(b, a, 0x05040100u); }
__device__ __forceinline__ unsigned perm_hi(unsigned a, unsigned b) { return __builtin_amdgcn_perm(b, a, 0x07060302u); }
__device__ __forceinline__ float asf(unsigned u) { return __builtin_bit_cast(float, u); }
__device__ __forceinline__ unsigned asu(float f) { return __builtin_bit_cast(unsigned, f); }
__device__ __forceinline__ unsigned cvtpk(float lo, float hi) {
  typedef __bf16 bf16x2 __attribute__((ext_vector_type(2)));
  bf16x2 r; r[0] = (__bf16)lo; r[1] = (__bf16)hi;
  return __builtin_bit_cast(unsigned, r);
}
__device__ __forceinline__ float rbf(float x) { return (float)(__bf16)x; }
template <int CTRL> __device__ __forceinline__ float dpp(float x) {
  return __builtin_bit_cast(float, __builtin_amdgcn_mov_dpp(__builtin_bit_cast(int, x), CTRL, 0xf, 0xf, true));
}
constexpr int DPP_XOR1 = 0xB1, DPP_XOR2 = 0x4E, DPP_ROR8 = 0x128;
__device__ __forceinline__ float x16_max(float x) {
  auto s = __builtin_amdgcn_permlane16_swap(asu(x), asu(x), false, false);
  return fmaxf(asf(s[0]), asf(s[1]));
}
__device__ __forceinline__ float x32_max(float x) {
  auto s = __builtin_amdgcn_permlane32_swap(asu(x), asu(x), false, false);
  return fmaxf(asf(s[0]), asf(s[1]));
}
__device__ __forceinline__ float x16_sum(float x) {
  auto s = __builtin_amdgcn_permlane16_swap(asu(x), asu(x), false, false);
  return asf(s[0]) + asf(s[1]);
}
__device__ __forceinline__ float x32_sum(float x) {
  auto s = __builtin_amdgcn_permlane32_swap(asu(x), asu(x), false, false);
  return asf(s[0]) + asf(s[1]);
}
__device__ __forceinline__ void wg_barrier() {
  asm volatile("s_waitcnt lgkmcnt(0)" ::: "memory");
  __builtin_amdgcn_s_barrier();
  asm volatile("" ::: "memory");
}
__device__ __forceinline__ void wait_until(unsigned due) {
  asm volatile("1:\n\ts_memrealtime vcc\n\ts_waitcnt lgkmcnt(0)\n\ts_sub_u32 vcc_lo, vcc_lo, %0\n\ts_cmp_lt_i32 vcc_lo, 0\n\ts_cbranch_scc0 2f\n\ts_sleep 1\n\ts_branch 1b\n2:"
               :: "s"(due) : "scc", "vcc", "memory");
}
#define PACE(n) do { if ((n) % LOCK == 0) __builtin_amdgcn_s_barrier(); WAIT_VM(WIN - 1); \
    if (((n) & 1) == 0) wait_until(t_start + (((n) - LEAD) * TAU16 >> 4)); } while (0)
#define SCHED_FENCE() __builtin_amdgcn_sched_barrier(0)
#define WAIT_VM(n) do { asm volatile("s_waitcnt vmcnt(%0)" ::"n"(n) : "memory"); } while (0)

template <int T0, int T1>
__device__ __forceinline__ void tr_mfma_part(const v4u& r0, const v4u& r1, const v4u& r2, const v4u& r3, const v4u& r4, const v4u& r5,
                                             const v4u& r6, const v4u& r7, v4u bfrag, f32x4 (&acc)[8]) {
#pragma unroll
  for (int t = T0; t < T1; ++t) {
    v4u lo, hi;
    lo[0] = perm_lo(r0[t], r1[t]); hi[0] = perm_hi(r0[t], r1[t]);
    lo[1] = perm_lo(r2[t], r3[t]); hi[1] = perm_hi(r2[t], r3[t]);
    lo[2] = perm_lo(r4[t], r5[t]); hi[2] = perm_hi(r4[t], r5[t]);
    lo[3] = perm_lo(r6[t], r7[t]); hi[3] = perm_hi(r6[t], r7[t]);
    acc[2 * t] = mfma(lo, bfrag, acc[2 * t]);
    acc[2 * t + 1] = mfma(hi, bfrag, acc[2 * t + 1]);
  }
}
#define TR_PART(T0, T1, a, o, bf, acc) tr_mfma_part<T0, T1>((a)[(o)], (a)[(o) + 1], (a)[(o) + 2], (a)[(o) + 3], (a)[(o) + 4], (a)[(o) + 5], (a)[(o) + 6], (a)[(o) + 7], bf, acc)
#define TR_MFMA8(a, o, bf, acc) TR_PART(0, 4, a, o, bf, acc)

__global__ void __launch_bounds__(NTHR, 2) fused_kernel(Params p) {
  __shared__ __attribute__((aligned(16))) unsigned char smem[L_END];
  LAS unsigned char* L = (LAS unsigned char*)smem;
  const int tid = threadIdx.x, lane = tid & 63;
  const int w = __builtin_amdgcn_readfirstlane(tid >> 6);
  const int wg = blockIdx.x;
  const int br = wg >> 7, b = (wg >> 5) & 3, kvh = (wg >> 3) & 3, sp = wg & 7;
  const int g = lane >> 4, c = lane & 15;
  const GAS unsigned char* wsb = (const GAS unsigned char*)p.ws;
  const __amdgpu_buffer_rsrc_t rs = __builtin_amdgcn_make_buffer_rsrc(p.ws, 0, WS_END, 0x00020000);

  const unsigned t_start = (unsigned)__builtin_amdgcn_s_memrealtime();
  const unsigned epoch = *(const gu32*)p.ws;
  unsigned cpf = 0, cpf2 = 0;
  if (w == 7) {
    const GAS unsigned char* pc = (const GAS unsigned char*)(__builtin_amdgcn_s_getpc() & ~127ull);
    cpf = *(const gu32*)(pc + lane * 128); cpf2 = *(const gu32*)(pc + 8192 + lane * 128);
  }

  const GAS unsigned char* xq = (const GAS unsigned char*)p.x;
  const int qidx = (((sp >> 1) + kvh + 2 * br) & 3) * 4 + b;
  const int cj = (int)((((sp & 1) ? 0xD5C49180u : 0xF7E6A2B3u) >> (4 * (qidx >> 1))) & 15u);
  const int kq = b & 1;
  const v4u xa = ldpl(xq + (((c >> 1) & 3) * DM + 512 * kq + 64 * w + 16 * g + 8 * (c & 1)) * 2);
  const int xk = 128 * w + 32 * g + 8 * (c & 3);
  const v4u xf = ldpl(xq + ((c >> 2) * DM + xk) * 2);
  const v4u gf = ldpl((const GAS unsigned char*)p.gw + xk * 2);
  const __amdgpu_buffer_rsrc_t rq = mkrsrc(br ? p.wq1 : p.wq0, 0, DM * NQ * 2);
  const int qvo = ((512 * kq + 64 * w + 8 * (lane >> 3)) * NQ + kvh * 1024 + cj * 64 + 8 * (lane & 7)) * 2;
  const size_t kvbase = ((size_t)(b * 4 + kvh) * SK + sp * 1024 + w * 16) * (DH * 2);
  const __amdgpu_buffer_rsrc_t rk = mkrsrc(br ? p.kc1 : p.kc0, kvbase, 7 * 32768 + 4096);
  const __amdgpu_buffer_rsrc_t rv = mkrsrc(br ? p.vc1 : p.vc0, kvbase, 7 * 32768 + 4096);
  const int rot = (wg >> 3) & 7;
  const int kvo = (c >> 2) * 256 + g * 64 + (c & 3) * 16, vvo = lane * 16;
#define LD_KROW(i) ldnt(rk, kvo + ((i) & 3) * 1024, ((((i) >> 2) + rot) & 7) * 32768)
#define LD_VROW(i) ldnt(rv, vvo + ((i) & 3) * 1024, ((((i) >> 2) + rot) & 7) * 32768)
  const int oh = (wg & 7) | ((wg >> 2) & 24), ocb = (wg >> 3) & 3;
  v4u wreg[8], kv[32];
#define ISSUE_HEAD(n) do { if ((n) < 8) wreg[n] = ldnt(rq, qvo, (n) * NQ * 2); else if ((n) < 40) kv[(n) - 8] = LD_KROW((n) - 8); \
    else wreg[(n) - 40] = LD_VROW((n) - 40); SCHED_FENCE(); } while (0)
  if (tid < 8) ((LAS unsigned*)(L + L_QF))[tid] = 0u;
#pragma unroll
  for (int n = 0; n < WIN; ++n) ISSUE_HEAD(n);
  wg_barrier();
  constexpr int QP_AT = 7 + WIN;
#pragma unroll
  for (int n = WIN; n <= QP_AT; ++n) { PACE(n); ISSUE_HEAD(n); }

  const unsigned tag = __builtin_amdgcn_readfirstlane(epoch) + 1u;
  {
    f32x4 acc[8];
#pragma unroll
    for (int j = 0; j < 8; ++j) acc[j] = f32x4{0.f, 0.f, 0.f, 0.f};
    TR_MFMA8(wreg, 0, xa, acc);
    if (c < 8 && (c & 1) == (g >> 1)) {
      LAS f32x4* qp = (LAS f32x4*)(L + L_QPART) + ((w * 2 + (g >> 1)) * 4 + (c >> 1)) * 16 + (g & 1) * 8;
#pragma unroll
      for (int r = 0; r < 4; ++r) {
        qp[2 * r] = f32x4{acc[0][r], acc[1][r], acc[2][r], acc[3][r]};
        qp[2 * r + 1] = f32x4{acc[4][r], acc[5][r], acc[6][r], acc[7][r]};
      }
    }
    float z = 0.f;
#pragma unroll
    for (int t = 0; t < 4; ++t) {
      z += rbf(asf(xf[t] << 16) * asf(gf[t] << 16));
      z += rbf(asf(xf[t] & 0xffff0000u) * asf(gf[t] & 0xffff0000u));
    }
    z += dpp<DPP_XOR1>(z); z += dpp<DPP_XOR2>(z); z = x16_sum(z); z = x32_sum(z);
    if (g == 0 && (c & 3) == 0) ((LAS float*)(L + L_ZP))[w * 4 + (c >> 2)] = z;
    asm volatile("s_waitcnt lgkmcnt(0)" ::: "memory");
    unsigned old = 0;
    if (lane == 0) old = __hip_atomic_fetch_add((LAS unsigned*)(L + L_QF + 16), 1u, __ATOMIC_RELAXED, __HIP_MEMORY_SCOPE_WORKGROUP);
    asm volatile("" :: "v"(cpf), "v"(cpf2));
    if (__builtin_amdgcn_readfirstlane(old) == 7u) {
      asm volatile("" ::: "memory");
#pragma unroll
      for (int h = 0; h < 2; ++h) {
        const int t = lane + 64 * h;
        const LAS f32x2* qp = (const LAS f32x2*)(L + L_QPART) + t;
        float s0 = 0.f, s1 = 0.f;
#pragma unroll
        for (int u = 0; u < 16; ++u) { const f32x2 v = qp[u * 128]; s0 += v[0]; s1 += v[1]; }
        v4u gr; gr[0] = asu(s0); gr[1] = tag; gr[2] = asu(s1); gr[3] = tag;
        __builtin_amdgcn_raw_buffer_store_b128(gr, rs, WS_QX + ((((br * 4 + kvh) * 4 + (t >> 5)) * 2 + kq) * 1024 + cj * 64 + 2 * (t & 31)) * 8, 0, AUX_SC1);
      }
      if (lane < 4) {
        float zz = 0.f;
#pragma unroll
        for (int u = 0; u < 8; ++u) zz += ((const LAS float*)(L + L_ZP))[u * 4 + lane];
        ((LAS float*)(L + L_GT))[lane] = rbf(1.0f / (1.0f + __expf(-rbf(zz))));
      }
    }
  }
  SCHED_FENCE();
  v4u qg[4];
  unsigned tw0 = 0, tw1 = 0;
  const __amdgpu_buffer_rsrc_t ro = mkrsrc(br ? p.wo1 : p.wo0, 0, NQ * DM * 2);
  bool ok = w >= 4;
  const int qoff = WS_QX + (((br * 4 + kvh) * 4 + b) * 2 * 1024 + (w & 3) * 256 + 2 * lane) * 8;
#define Q_LOADS() do { asm volatile("" ::: "memory"); _Pragma("unroll") for (int u_ = 0; u_ < 4; ++u_) qg[u_] = __builtin_amdgcn_raw_buffer_load_b128(rs, qoff + (u_ & 1) * 8192 + (u_ >> 1) * 1024, 0, AUX_SC1); } while (0)
#define Q_TEST() do { bool t_ = true; _Pragma("unroll") for (int u_ = 0; u_ < 4; ++u_) t_ = t_ & (qg[u_][1] == tag) & (qg[u_][3] == tag); \
      const bool a_ = __all(t_); \
      if (!ok && a_) { ok = true; \
        *(LAS unsigned*)(L + L_QS + w * 512 + lane * 4) = cvtpk(asf(qg[0][0]) + asf(qg[1][0]), asf(qg[0][2]) + asf(qg[1][2])); \
        *(LAS unsigned*)(L + L_QS + w * 512 + 256 + lane * 4) = cvtpk(asf(qg[2][0]) + asf(qg[3][0]), asf(qg[2][2]) + asf(qg[3][2])); \
        asm volatile("" ::: "memory"); \
        if (lane == 0) *(volatile LAS unsigned*)(L + L_QF + w * 4) = 1u; } } while (0)
#define Q_POLL() do { if (!ok) Q_LOADS(); SCHED_FENCE(); } while (0)
#define Q_CHECK() do { if (w < 4) Q_TEST(); SCHED_FENCE(); } while (0)
#pragma unroll
  for (int n = QP_AT + 1; n < 48; ++n) {
    PACE(n);
    if (n == POLL0 || n == POLL0 + 8 || n == POLL0 + 16) Q_POLL();
    if (n == 24 && w == 5) tw0 = __builtin_amdgcn_raw_buffer_load_b32(rv, 0, 0, AUX_NT);
    if (n == 32 && w == 6) tw1 = __builtin_amdgcn_raw_buffer_load_b32(ro, oh * 128 * DM * 2, 0, AUX_NT);
    ISSUE_HEAD(n);
    if (n == POLL0 + 7 || n == POLL0 + 15 || n == POLL0 + 23) Q_CHECK();
  }
  if (w < 4) for (unsigned spins = 0; !ok && spins < SPIN_CAP; ++spins) { Q_LOADS(); Q_TEST(); }
#pragma unroll 1
  for (unsigned spins = 0; spins < SPIN_CAP; ++spins) {
    const v4u fl = *(volatile LAS v4u*)(L + L_QF);
    if (__builtin_amdgcn_readfirstlane(fl[0] & fl[1] & fl[2] & fl[3])) break;
    __builtin_amdgcn_s_sleep(1);
  }
  asm volatile("" ::: "memory");

  const v4u qb1 = *(const LAS v4u*)(L + L_QS + (c & 7) * 256 + (4 * g + (c >> 3)) * 16);
  const v4u qb2 = *(const LAS v4u*)(L + L_QS + (c & 7) * 256 + (4 * g + 2 + (c >> 3)) * 16);
  const bool lo8 = c < 8;
  float S[16];
  const f32x4 zero4 = f32x4{0.f, 0.f, 0.f, 0.f};
  v4u vb[24];
#pragma unroll
  for (int pr = 0; pr < 16; ++pr) {
    PACE(48 + pr);
    vb[pr] = LD_VROW(8 + pr);
    SCHED_FENCE();
    const f32x4 a1 = mfma(kv[2 * pr], qb1, zero4), a2 = mfma(kv[2 * pr], qb2, zero4);
    const f32x4 b1 = mfma(kv[2 * pr + 1], qb1, zero4), b2 = mfma(kv[2 * pr + 1], qb2, zero4);
    const float own = lo8 ? (a1[0] + a2[2]) : (b1[1] + b2[3]);
    const float oth = lo8 ? (b1[0] + b2[2]) : (a1[1] + a2[3]);
    S[pr] = rbf(rbf(own + dpp<DPP_ROR8>(oth)) * 0.08838834764831845f);
    SCHED_FENCE();
  }
  asm volatile("" :: "v"(tw0), "v"(tw1));
  const int ovo = ((oh * 128 + 32 * (w >> 1) + 2 * g) * DM + ocb * 256 + 128 * (w & 1) + 8 * c) * 2;
  v4u wt[8];
#define LD_V(i) do { SCHED_FENCE(); PACE(40 + (i)); vb[(i) - 8] = LD_VROW(i); SCHED_FENCE(); } while (0)
#define LD_WO(i) do { SCHED_FENCE(); PACE(72 + (i)); wt[i] = ldnt(ro, ovo, (8 * ((i) & 3) + ((i) >> 2)) * DM * 2); SCHED_FENCE(); } while (0)
  constexpr float LOG2E = 1.4426950408889634f;
  LD_V(24);
  float m = S[0];
#pragma unroll
  for (int pr = 1; pr < 16; ++pr) m = fmaxf(m, S[pr]);
  m = fmaxf(m, dpp<DPP_ROR8>(m)); m = x16_max(m); m = x32_max(m);
  v4u pb[4];
  LD_V(25);
#pragma unroll
  for (int pr = 0; pr < 16; ++pr) {
    const float pv = __builtin_amdgcn_exp2f((S[pr] - m) * LOG2E), px = dpp<DPP_ROR8>(pv);
    pb[pr >> 2][pr & 3] = cvtpk(lo8 ? pv : px, lo8 ? px : pv);
  }
  f32x4 lacc = zero4;
  const v4u ones = v4u{0x3f803f80u, 0x3f803f80u, 0x3f803f80u, 0x3f803f80u};
#pragma unroll
  for (int be = 0; be < 4; ++be) lacc = mfma(ones, pb[be], lacc);
  if (lo8 && g == 0) *(LAS f32x2*)(L + L_ML + (w * 8 + c) * 8) = f32x2{m * LOG2E, lacc[0]};
  f32x4 acc[8];
#pragma unroll
  for (int j = 0; j < 8; ++j) acc[j] = zero4;
  LD_V(26);
  TR_PART(0, 2, wreg, 0, pb[0], acc);
  LD_V(27);
  TR_PART(2, 4, wreg, 0, pb[0], acc);
  wg_barrier();
  float msc[8], lse;
  {
    float mw[8], lw[8], M = -INFINITY, Ls = 0.f;
#pragma unroll
    for (int u = 0; u < 8; ++u) { const f32x2 v = *(const LAS f32x2*)(L + L_ML + (u * 8 + w) * 8); mw[u] = v[0]; lw[u] = v[1]; M = fmaxf(M, v[0]); }
#pragma unroll
    for (int u = 0; u < 8; ++u) { msc[u] = __builtin_amdgcn_exp2f(mw[u] - M); Ls = fmaf(msc[u], lw[u], Ls); }
    const float inv = __builtin_amdgcn_rcpf(Ls);
#pragma unroll
    for (int u = 0; u < 8; ++u) msc[u] *= inv;
    lse = M + __builtin_amdgcn_logf(Ls);
  }
  LD_V(28);
  TR_PART(0, 2, vb, 0, pb[1], acc);
  LD_V(29);
  TR_PART(2, 4, vb, 0, pb[1], acc);
  LD_V(30);
  LD_V(31);
  TR_PART(0, 2, vb, 8, pb[2], acc);
  LD_WO(0);
  TR_PART(2, 4, vb, 8, pb[2], acc);
  LD_WO(1); LD_WO(2); LD_WO(3); LD_WO(4); LD_WO(5); LD_WO(6); LD_WO(7);
  TR_MFMA8(vb, 16, pb[3], acc);
  {
    LAS f32x4* op = (LAS f32x4*)(L + L_OPART + ((w * 8 + (c & 7)) * OP_STR + 32 * g) * 4) + (lo8 ? 0 : 4);
#pragma unroll
    for (int j = 0; j < 4; ++j) {
      f32x4 v;
#pragma unroll
      for (int e = 0; e < 4; ++e) v[e] = lo8 ? acc[j][e] : acc[j + 4][e];
      op[j] = v;
    }
  }
  wg_barrier();
  {
    float o0 = 0.f, o1 = 0.f;
#pragma unroll
    for (int u = 0; u < 8; ++u) {
      const f32x2 v = *(const LAS f32x2*)(L + L_OPART + ((u * 8 + w) * OP_STR + 2 * lane) * 4);
      o0 = fmaf(msc[u], v[0], o0); o1 = fmaf(msc[u], v[1], o1);
    }
    v2u gr; gr[0] = cvtpk(o0, o1); gr[1] = tag;
    __builtin_amdgcn_raw_buffer_store_b64(gr, rs, WS_PX + (wg * 8 + w) * PX_REC + lane * 8, 0, AUX_SC1);
    if (lane < 16) {
      v2u lg; lg[0] = asu(lse); lg[1] = tag;
      __builtin_amdgcn_raw_buffer_store_b64(lg, rs, WS_PX + (wg * 8 + w) * PX_REC + 512 + lane * 8, 0, AUX_SC1);
    }
  }
  v4u wa[8];
  {
    const int ob = w >> 1, hf = w & 1;
    const int prec = WS_PX + ((((br * 4 + ob) * 4 + (oh >> 3)) * 8 + g) * 8 + (oh & 7)) * PX_REC;
    const int pbase = prec + hf * 256 + c * 16;
    const int lbase = prec + 512;
    v4u pv0, pv1; v2u lg0, lg1;
    WAIT_VM(GATHER_AT + 2);
    pv0 = __builtin_amdgcn_raw_buffer_load_b128(rs, pbase, 0, AUX_SC1);
    pv1 = __builtin_amdgcn_raw_buffer_load_b128(rs, pbase + 4 * 8 * PX_REC, 0, AUX_SC1);
    lg0 = __builtin_amdgcn_raw_buffer_load_b64(rs, lbase, 0, AUX_SC1);
    lg1 = __builtin_amdgcn_raw_buffer_load_b64(rs, lbase + 4 * 8 * PX_REC, 0, AUX_SC1);
    SCHED_FENCE();
#pragma unroll
    for (int t = 0; t < 4; ++t)
#pragma unroll
      for (int u = 0; u < 4; ++u) { wa[2 * t][u] = perm_lo(wt[2 * u][t], wt[2 * u + 1][t]); wa[2 * t + 1][u] = perm_hi(wt[2 * u][t], wt[2 * u + 1][t]); }
    asm volatile("" : "+v"(wa[0]), "+v"(wa[1]), "+v"(wa[2]), "+v"(wa[3]), "+v"(wa[4]), "+v"(wa[5]), "+v"(wa[6]), "+v"(wa[7]));
    SCHED_FENCE();
    for (unsigned spins = 0; spins < SPIN_CAP; ++spins) {
      const bool lane_ok = (lg0[1] == tag) & (lg1[1] == tag) & (pv0[1] == tag) & (pv0[3] == tag) & (pv1[1] == tag) & (pv1[3] == tag);
      unsigned long long miss = __ballot(!lane_ok);
      if (miss == 0ull) break;
      while (__builtin_popcountll(miss) > 16 && ++spins < SPIN_CAP) {
        __builtin_amdgcn_s_sleep(2);
        asm volatile("" ::: "memory");
        const v2u t0 = __builtin_amdgcn_raw_buffer_load_b64(rs, lbase, 0, AUX_SC1);
        const v2u t1 = __builtin_amdgcn_raw_buffer_load_b64(rs, lbase + 4 * 8 * PX_REC, 0, AUX_SC1);
        miss = __ballot(!((t0[1] == tag) & (t1[1] == tag)));
      }
      asm volatile("" ::: "memory");
      if (!lane_ok) {
        pv0 = __builtin_amdgcn_raw_buffer_load_b128(rs, pbase, 0, AUX_SC1);
        pv1 = __builtin_amdgcn_raw_buffer_load_b128(rs, pbase + 4 * 8 * PX_REC, 0, AUX_SC1);
        lg0 = __builtin_amdgcn_raw_buffer_load_b64(rs, lbase, 0, AUX_SC1);
        lg1 = __builtin_amdgcn_raw_buffer_load_b64(rs, lbase + 4 * 8 * PX_REC, 0, AUX_SC1);
      }
    }
    const float l0 = asf(lg0[0]), l1 = asf(lg1[0]);
    const float M = x32_max(x16_max(fmaxf(l0, l1)));
    const float w0 = __builtin_amdgcn_exp2f(l0 - M), w1 = __builtin_amdgcn_exp2f(l1 - M);
    const float inv = __builtin_amdgcn_rcpf(x32_sum(x16_sum(w0 + w1)));
    const float f0 = w0 * inv, f1 = w1 * inv;
    float o[4];
    o[0] = f0 * asf(pv0[0] << 16) + f1 * asf(pv1[0] << 16);
    o[1] = f0 * asf(pv0[0] & 0xffff0000u) + f1 * asf(pv1[0] & 0xffff0000u);
    o[2] = f0 * asf(pv0[2] << 16) + f1 * asf(pv1[2] << 16);
    o[3] = f0 * asf(pv0[2] & 0xffff0000u) + f1 * asf(pv1[2] & 0xffff0000u);
#pragma unroll
    for (int e = 0; e < 4; ++e) o[e] = x32_sum(x16_sum(o[e]));
    if (lane < 16) *(LAS v2u*)(L + L_OB + ob * 256 + hf * 128 + lane * 8) = v2u{cvtpk(o[0], o[1]), cvtpk(o[2], o[3])};
  }
  wg_barrier();
  {
    const v4u ob = *(const LAS v4u*)(L + L_OB + (c & 3) * 256 + (32 * (w >> 1) + 8 * g) * 2);
    f32x4 oa[8];
#pragma unroll
    for (int j = 0; j < 8; ++j) oa[j] = mfma(wa[j], ob, zero4);
    {
      const int q = c >> 2;
      LAS f32x4* op = (LAS f32x4*)(L + L_WOP + (((w >> 1) * 4 + (c & 3)) * 260 + 128 * (w & 1) + 32 * g) * 4) + 2 * q;
      f32x4 x[4];
#pragma unroll
      for (int k = 0; k < 4; ++k)
#pragma unroll
        for (int e = 0; e < 4; ++e) x[k][e] = q < 2 ? oa[k][e] : oa[k + 4][e];
#pragma unroll
      for (int k = 0; k < 2; ++k) {
        f32x4 y;
#pragma unroll
        for (int e = 0; e < 4; ++e) y[e] = (q & 1) ? x[k + 2][e] : x[k][e];
        op[k] = y;
      }
    }
  }
  wg_barrier();
  {
    const int r = tid >> 3, ob = (tid >> 1) & 3, cp = tid & 1;
    const int col = 4 * r + 2 * cp;
    const int pos = (col & ~31) + 4 * (col & 7) + ((col >> 3) & 3);
    float s0 = 0.f, s1 = 0.f;
#pragma unroll
    for (int u = 0; u < 4; ++u) {
      const LAS float* q = (const LAS float*)(L + L_WOP) + (u * 4 + ob) * 260 + pos;
      s0 += q[0]; s1 += q[4];
    }
    v2u gr; gr[0] = cvtpk(s0, s1); gr[1] = tag;
    __builtin_amdgcn_raw_buffer_store_b64(gr, rs, WS_OX + (ocb * 64 + br * 32 + oh) * 4096 + ((r + 2 * (br * 32 + oh)) & 63) * 64 + (tid & 7) * 8, 0, AUX_SC1);
  }
  {
    const int r = br * 32 + oh;
    const int obase = WS_OX + (ocb * 64 + 8 * w + (lane >> 3)) * 4096 + ((r + 2 * (8 * w + (lane >> 3))) & 63) * 64 + (lane & 7) * 8;
    WAIT_VM(0);
    v2u pa;
    for (unsigned spins = 0; spins < SPIN_CAP; ++spins) {
      asm volatile("" ::: "memory");
      pa = __builtin_amdgcn_raw_buffer_load_b64(rs, obase, 0, AUX_SC1);
      if (__all(pa[1] == tag)) break;
    }
    float s0 = asf(pa[0] << 16), s1 = asf(pa[0] & 0xffff0000u);
    s0 += dpp<DPP_ROR8>(s0); s1 += dpp<DPP_ROR8>(s1);
    s0 = x32_sum(x16_sum(s0)); s1 = x32_sum(x16_sum(s1));
    if (lane < 8) *(LAS f32x2*)(L + L_ML + (w * 8 + lane) * 8) = f32x2{s0, s1};
    asm volatile("s_waitcnt lgkmcnt(0)" ::: "memory");
    unsigned old = 0;
    if (lane == 0) old = __hip_atomic_fetch_add((LAS unsigned*)(L + L_QF + 20), 1u, __ATOMIC_RELAXED, __HIP_MEMORY_SCOPE_WORKGROUP);
    if (__builtin_amdgcn_readfirstlane(old) == 7u) {
      asm volatile("" ::: "memory");
      if (lane < 8) {
        float a0 = 0.f, a1 = 0.f, b0 = 0.f, b1 = 0.f;
#pragma unroll
        for (int u = 0; u < 4; ++u) {
          const f32x2 va = *(const LAS f32x2*)(L + L_ML + (u * 8 + lane) * 8), vb2 = *(const LAS f32x2*)(L + L_ML + ((u + 4) * 8 + lane) * 8);
          a0 += va[0]; a1 += va[1]; b0 += vb2[0]; b1 += vb2[1];
        }
        const float gate = ((const LAS float*)(L + L_GT))[lane >> 1];
        const float og = rbf(1.0f - gate);
        float ta0 = rbf(gate * rbf(a0)), ta1 = rbf(gate * rbf(a1)), tb0 = rbf(og * rbf(b0)), tb1 = rbf(og * rbf(b1));
        asm volatile("" : "+v"(ta0), "+v"(ta1), "+v"(tb0), "+v"(tb1));
        ((GAS unsigned*)p.out)[((lane >> 1) * DM + ocb * 256 + 4 * r + 2 * (lane & 1)) >> 1] = cvtpk(ta0 + tb0, ta1 + tb1);
        if (wg == 0 && lane == 0) __hip_atomic_store((gu32*)p.ws, tag, __ATOMIC_RELAXED, __HIP_MEMORY_SCOPE_AGENT);
      }
    }
  }
}

extern "C" void kernel_launch(void* const* d_in, const int* in_sizes, int n_in, void* d_out, int out_size, void* d_ws,
                              size_t ws_size, hipStream_t stream) {
  Params p;
  p.x = d_in[0];
  p.kc0 = d_in[1]; p.vc0 = d_in[2]; p.wq0 = d_in[3]; p.wo0 = d_in[4];
  p.kc1 = d_in[6]; p.vc1 = d_in[7]; p.wq1 = d_in[8]; p.wo1 = d_in[9];
  p.gw = d_in[10];
  p.out = d_out;
  p.ws = d_ws;
  hipLaunchKernelGGL(fused_kernel, dim3(NWG), dim3(NTHR), 0, stream, p);
}
```
